```python
import torch

def _softmax(x, axis=-1):
    return torch.softmax(x, dim=axis)

def _gelu(x):
    return torch.nn.functional.gelu(x, approximate='tanh')

def _silu(x):
    return torch.nn.functional.silu(x)

def _sigmoid(x):
    return torch.sigmoid(x)

def _layernorm(x, axis=-1, eps=1e-05):
    m = x.mean(dim=axis, keepdim=True)
    v = x.var(dim=axis, keepdim=True, unbiased=False)
    return (x - m) / torch.sqrt(v + eps)

def _scatter_add(tgt, idx, src, axis=0):
    return tgt.clone().index_add_(axis, idx, src)

def _segment_sum(src, seg, n):
    out = torch.zeros((n,) + src.shape[1:], dtype=src.dtype)
    return out.index_add_(0, seg, src)

def _pad(x, pw):
    flat = tuple((v for pair in reversed(pw) for v in pair))
    return torch.nn.functional.pad(x, flat)
kernel_name = 'gated_mix__memory_aug_recurrent_mem_tr__memory_aug_recurrent_mem_tr__prompt'

def setup_inputs():

    _w = lambda a: (lambda t: t.to(torch.bfloat16) if t.is_floating_point() else t)(torch.as_tensor(a))

    def _A_setup():
        B, S, Dm = (1, 2048, 576)
        H, Dh = (32, 128)
        M, seg = (8, 32)
        seg = min(seg, S)
        S = S // seg * seg
        return {'x': _w((lambda *s: torch.randn(*s, dtype=torch.bfloat16) * 0.05)(B, S, Dm)), 'mem0': _w((lambda *s: torch.randn(*s, dtype=torch.bfloat16) * 0.4)(B, M, Dm)), 'Wq': _w((lambda *s: torch.randn(*s, dtype=torch.bfloat16) * 6.4)(Dm, H, Dh) * Dm ** (-0.5)), 'Wk': _w((lambda *s: torch.randn(*s, dtype=torch.bfloat16) * 6.4)(Dm, H, Dh) * Dm ** (-0.5)), 'Wv': _w((lambda *s: torch.randn(*s, dtype=torch.bfloat16) * 0.4)(Dm, H, Dh) * Dm ** (-0.5)), 'Wo': _w((lambda *s: torch.randn(*s, dtype=torch.bfloat16) * 0.4)(H, Dh, Dm) * (H * Dh) ** (-0.5)), 'seg_len': seg}
    _A_in = _A_setup()

    def _B_setup():
        B, S, Dm = (1, 2048, 576)
        H, Dh = (32, 128)
        M, seg = (8, 32)
        seg = min(seg, S)
        S = S // seg * seg
        return {'x': _w((lambda *s: torch.randn(*s, dtype=torch.bfloat16) * 0.05)(B, S, Dm)), 'mem0': _w((lambda *s: torch.randn(*s, dtype=torch.bfloat16) * 0.4)(B, M, Dm)), 'Wq': _w((lambda *s: torch.randn(*s, dtype=torch.bfloat16) * 6.4)(Dm, H, Dh) * Dm ** (-0.5)), 'Wk': _w((lambda *s: torch.randn(*s, dtype=torch.bfloat16) * 6.4)(Dm, H, Dh) * Dm ** (-0.5)), 'Wv': _w((lambda *s: torch.randn(*s, dtype=torch.bfloat16) * 0.4)(Dm, H, Dh) * Dm ** (-0.5)), 'Wo': _w((lambda *s: torch.randn(*s, dtype=torch.bfloat16) * 0.4)(H, Dh, Dm) * (H * Dh) ** (-0.5)), 'seg_len': seg}
    _B_in = _B_setup()
    return {'A_x': _A_in['x'], 'A_mem0': _A_in['mem0'], 'A_Wq': _A_in['Wq'], 'A_Wk': _A_in['Wk'], 'A_Wv': _A_in['Wv'], 'A_Wo': _A_in['Wo'], 'A_seg_len': _A_in['seg_len'], 'B_x': _B_in['x'], 'B_mem0': _B_in['mem0'], 'B_Wq': _B_in['Wq'], 'B_Wk': _B_in['Wk'], 'B_Wv': _B_in['Wv'], 'B_Wo': _B_in['Wo'], 'B_seg_len': _B_in['seg_len'], 'gate_w': _w((lambda *s: torch.randn(*s, dtype=torch.bfloat16) * 0.05)(576) * 16.0)}

def reference(A_x, A_mem0, A_Wq, A_Wk, A_Wv, A_Wo, A_seg_len, B_x, B_mem0, B_Wq, B_Wk, B_Wv, B_Wo, B_seg_len, gate_w):

    def _A_ref(x, mem0, Wq, Wk, Wv, Wo, seg_len):
        B, S, Dm = x.shape
        H, Dh = (Wq.shape[1], Wq.shape[2])
        M = mem0.shape[1]
        scale = Dh ** (-0.5)
        n_seg = S // seg_len
        L = M + seg_len
        idx = torch.arange(L)
        mask = (idx[:, None] < M) | (idx[:, None] >= idx[None, :])
        mem = mem0
        outs = []
        for s in range(n_seg):
            xs = x[:, s * seg_len:(s + 1) * seg_len, :].clone()
            h = torch.concatenate([mem, xs], axis=1)
            q = torch.einsum('btd,dhe->bhte', h, Wq)
            k = torch.einsum('btd,dhe->bhte', h, Wk)
            v = torch.einsum('btd,dhe->bhte', h, Wv)
            scores = torch.einsum('bhqe,bhke->bhqk', q, k) * scale
            scores = torch.where(mask[None, None], scores, -1000000000.0)
            p = _softmax(scores, axis=-1)
            o = torch.einsum('bhqk,bhke->bhqe', p, v)
            o = torch.einsum('bhte,hed->btd', o, Wo)
            mem = o[:, :M, :].clone()
            outs.append(o[:, M:, :])
        return torch.concatenate(outs, axis=1)

    def _B_ref(x, mem0, Wq, Wk, Wv, Wo, seg_len):
        B, S, Dm = x.shape
        H, Dh = (Wq.shape[1], Wq.shape[2])
        M = mem0.shape[1]
        scale = Dh ** (-0.5)
        n_seg = S // seg_len
        L = M + seg_len
        idx = torch.arange(L)
        mask = (idx[:, None] < M) | (idx[:, None] >= idx[None, :])
        mem = mem0
        outs = []
        for s in range(n_seg):
            xs = x[:, s * seg_len:(s + 1) * seg_len, :].clone()
            h = torch.concatenate([mem, xs], axis=1)
            q = torch.einsum('btd,dhe->bhte', h, Wq)
            k = torch.einsum('btd,dhe->bhte', h, Wk)
            v = torch.einsum('btd,dhe->bhte', h, Wv)
            scores = torch.einsum('bhqe,bhke->bhqk', q, k) * scale
            scores = torch.where(mask[None, None], scores, -1000000000.0)
            p = _softmax(scores, axis=-1)
            o = torch.einsum('bhqk,bhke->bhqe', p, v)
            o = torch.einsum('bhte,hed->btd', o, Wo)
            mem = o[:, :M, :].clone()
            outs.append(o[:, M:, :])
        return torch.concatenate(outs, axis=1)
    x = A_x
    g = _sigmoid((x * gate_w).sum(-1, keepdims=True))
    a = _A_ref(x, A_mem0, A_Wq, A_Wk, A_Wv, A_Wo, A_seg_len)
    b = _B_ref(x, B_mem0, B_Wq, B_Wk, B_Wv, B_Wo, B_seg_len)
    return g * a + (1 - g) * b
```

```cpp
#include <hip/hip_runtime.h>
#include <stdint.h>
#define TOUCH 0
#define WOCOPT 1

typedef __bf16 bf16x8 __attribute__((ext_vector_type(8)));
typedef __bf16 bf16x2 __attribute__((ext_vector_type(2)));
typedef short s16x4 __attribute__((ext_vector_type(4)));
typedef float f32x4 __attribute__((ext_vector_type(4)));
typedef float f32x2 __attribute__((ext_vector_type(2)));
typedef unsigned u32x4 __attribute__((ext_vector_type(4)));
typedef unsigned u32x2 __attribute__((ext_vector_type(2)));
#define LAS __attribute__((address_space(3)))
#define GAS __attribute__((address_space(1)))
typedef GAS unsigned gu32;
#define RLX_AGENT __ATOMIC_RELAXED, __HIP_MEMORY_SCOPE_AGENT
#define DEV __device__ __forceinline__

constexpr unsigned CTL_BYTES = 98304;
constexpr unsigned GRP_OFF = 0, POL_OFF = 512, TICK_OFF = 768, GATEC_OFF = 896, WOCC_OFF = 960, KVC_OFF = 1024, KMC_OFF = 32768, TQ_OFF = 49152, ERR_OFF = 61440, OC_OFF = 65536;
constexpr unsigned GATE = 0x80000;
constexpr unsigned XMIN = 0x100000;
constexpr unsigned KVM = 0x800000;
constexpr unsigned KV = 0x1800000;
constexpr unsigned OB = 0x7800000;
constexpr unsigned WOC = 0x9800000;
constexpr unsigned WS_BYTES = 0xa200000;
constexpr int LDS_BYTES = 163840;
constexpr unsigned long long P1_DELAY = 1000, RG_DELAY = 800;
constexpr unsigned SPIN_CAP = 1u << 20;

struct Params { const void* p[14]; };

DEV unsigned pk_bf16(float a, float b) { bf16x2 v = {(__bf16)a, (__bf16)b}; return __builtin_bit_cast(unsigned, v); }
DEV float rbf(float a) { return (float)(__bf16)a; }
DEV float bf_lo(unsigned u) { return __builtin_bit_cast(float, u << 16); }
DEV float bf_hi(unsigned u) { return __builtin_bit_cast(float, u & 0xffff0000u); }
DEV f32x4 mfma32(u32x4 a, u32x4 b, f32x4 c) { return __builtin_amdgcn_mfma_f32_16x16x32_bf16(__builtin_bit_cast(bf16x8, a), __builtin_bit_cast(bf16x8, b), c, 0, 0, 0); }
DEV f32x4 mfma16(u32x2 a, u32x2 b, f32x4 c) { return __builtin_amdgcn_mfma_f32_16x16x16bf16_1k(__builtin_bit_cast(s16x4, a), __builtin_bit_cast(s16x4, b), c, 0, 0, 0); }
DEV u32x2 tr_read(unsigned addr) { return __builtin_bit_cast(u32x2, __builtin_amdgcn_ds_read_tr16_b64_v4i16((LAS s16x4*)addr)); }
DEV u32x4 lds_rd128(unsigned addr) { return *(LAS u32x4*)addr; }
DEV void lds_wr128(unsigned addr, u32x4 v) { *(LAS u32x4*)addr = v; }
DEV void dma16(__amdgpu_buffer_rsrc_t r, unsigned voff, unsigned soff, unsigned lds) {
  asm volatile("s_mov_b32 m0, %0\n\ts_nop 0\n\tbuffer_load_dwordx4 %1, %2, %3 offen lds" :: "s"(lds), "v"(voff), "s"(r), "s"(soff) : "memory");
}
DEV void dma16_sc1(__amdgpu_buffer_rsrc_t r, unsigned voff, unsigned soff, unsigned lds) {
  asm volatile("s_mov_b32 m0, %0\n\ts_nop 0\n\tbuffer_load_dwordx4 %1, %2, %3 offen sc1 lds" :: "s"(lds), "v"(voff), "s"(r), "s"(soff) : "memory");
}
DEV void st128(u32x4 v, __amdgpu_buffer_rsrc_t r, unsigned voff, unsigned soff) {
  __builtin_amdgcn_raw_buffer_store_b128(v, r, voff, soff, 16);
  asm volatile("s_nop 1" :: "v"(v));
}
#define WAIT_VM(n) asm volatile("s_waitcnt vmcnt(" #n ")" ::: "memory")
#define MFMA_DONE() do { __builtin_amdgcn_sched_barrier(0); asm volatile("s_nop 7\n\ts_nop 1"); __builtin_amdgcn_sched_barrier(0); } while (0)
#define WAIT_LGKM0() asm volatile("s_waitcnt lgkmcnt(0)" ::: "memory")
DEV void bar() { asm volatile("s_waitcnt lgkmcnt(0)\n\ts_barrier" ::: "memory"); }
DEV __amdgpu_buffer_rsrc_t mkrsrc(const void* p, unsigned bytes) { return __builtin_amdgcn_make_buffer_rsrc((void*)p, 0, bytes, 0x00020000); }

#define TLS(prm, slot) do {} while (0)
#define TLX(prm, slot) do {} while (0)
DEV void wait_ge(gu32* ctl, unsigned off, unsigned want) {
  gu32* w = ctl + off / 4;
  for (unsigned sp = 0; __hip_atomic_load(w, RLX_AGENT) < want; ++sp) {
    __builtin_amdgcn_s_sleep(2);
    if (sp > SPIN_CAP) { __hip_atomic_store(ctl + ERR_OFF / 4, 1u, RLX_AGENT); break; }
    if ((sp & 1023u) == 1023u && __hip_atomic_load(ctl + ERR_OFF / 4, RLX_AGENT)) break;
  }
}

DEV void stage_w(const void* W, int h, int half, int wave, int lane) {
  const __amdgpu_buffer_rsrc_t r = mkrsrc((const char*)W + h * 256, 575u * 8192u + 256u);
  const int rr = lane >> 4, cc = lane & 15;
  const unsigned base = rr * 8192 + (cc & 1) * 16;
  const unsigned p0 = base + (((cc >> 1) ^ rr ^ ((wave & 1) << 2)) << 5), p1 = p0 ^ 128u;
  unsigned so = (unsigned)(half * 288 + wave * 72) * 8192u, ld = (unsigned)half * 73728u + (unsigned)wave * 18432u;
#pragma unroll 1
  for (int i = 0; i < 4; ++i) {
    dma16(r, p0, so, ld); dma16(r, p0, so + 32768u, ld + 1024u); dma16(r, p1, so + 65536u, ld + 2048u); dma16(r, p1, so + 98304u, ld + 3072u);
    so += 131072u; ld += 4096u;
  }
  dma16(r, p0, so, ld); dma16(r, p0, so + 32768u, ld + 1024u);
}
DEV void read_w(u32x4 (&f)[2][18], int half, int wave, int lane) {
  const int g = lane >> 4, q = (lane >> 2) & 3, p = lane & 3;
#pragma unroll
  for (int t = 0; t < 2; ++t) {
    const unsigned b0 = (8 * g + q) * 256 + (((2 * wave + t) ^ (q | ((g & 1) << 2))) << 5) + 8 * p;
#pragma unroll
    for (int k = 0; k < 9; ++k) {
      const int ks = 9 * half + k;
      const u32x2 lo = tr_read(b0 + ks * 8192), hi = tr_read(b0 + ks * 8192 + 1024);
      f[t][ks] = u32x4{lo.x, lo.y, hi.x, hi.y};
    }
  }
}
struct XStage {
  unsigned voff[9]; unsigned rd_e, rd_o;
  DEV void init(int wave, int lane) {
#pragma unroll
    for (int i = 0; i < 9; ++i) { const int P = (wave * 9 + i) * 64 + lane, row = P / 72, c = P - row * 72; voff[i] = row * 1152 + ((c ^ (row & 7)) << 4); }
    const int i16 = lane & 15, g = lane >> 4;
    rd_e = i16 * 1152 + ((g ^ (i16 & 7)) << 4); rd_o = i16 * 1152 + (((4 + g) ^ (i16 & 7)) << 4);
  }
  DEV void issue(__amdgpu_buffer_rsrc_t rx, int s, unsigned buf, int wave) const {
    const unsigned so = (unsigned)s * 36864u, ld = buf + (unsigned)wave * 9216u;
#pragma unroll
    for (int i = 0; i < 9; ++i) dma16(rx, voff[i], so, ld + i * 1024u);
  }
  DEV void issue1(__amdgpu_buffer_rsrc_t rx, int s, unsigned buf, int wave, int i) const { dma16(rx, voff[i], (unsigned)s * 36864u, buf + (unsigned)wave * 9216u + i * 1024u); }
  DEV u32x4 frag(unsigned buf, int T, int ks) const { return lds_rd128(buf + ((ks & 1) ? rd_o : rd_e) + T * 18432 + (ks >> 1) * 128); }
};

template <bool KVROLE>
DEV void role_p1(const Params& prm, int b, int h, int wave, int lane, int tl_id) {
  gu32* ctl = (gu32*)prm.p[10];
  const __amdgpu_buffer_rsrc_t rws = mkrsrc(prm.p[10], WS_BYTES), rx = mkrsrc(prm.p[11], 2048u * 1152u);
  u32x4 wa[2][18], wb[2][18];
  const void* Wa = prm.p[b * 5 + (KVROLE ? 1 : 0)];
  stage_w(Wa, h, 0, wave, lane); stage_w(Wa, h, 1, wave, lane);
  XStage xs; xs.init(wave, lane);
  if (KVROLE) {
    WAIT_VM(18); bar(); read_w(wa, 0, wave, lane); bar(); stage_w(prm.p[b * 5 + 2], h, 0, wave, lane);
    WAIT_VM(18); bar(); read_w(wa, 1, wave, lane); bar(); stage_w(prm.p[b * 5 + 2], h, 1, wave, lane);
    WAIT_VM(18); bar(); read_w(wb, 0, wave, lane); bar(); xs.issue(rx, 0, 0, wave);
    WAIT_VM(9); bar(); read_w(wb, 1, wave, lane); bar();
  } else {
    WAIT_VM(18); bar(); read_w(wa, 0, wave, lane); bar(); xs.issue(rx, 0, 0, wave);
    WAIT_VM(9); bar(); read_w(wa, 1, wave, lane); bar();
  }
  TLS(prm, 1);
  gu32* cnt = ctl + (KVC_OFF + ((b * 32 + h) * 2 + (KVROLE ? 0 : 1)) * 128) / 4;
  const unsigned l16 = lane * 16;
  constexpr int NST = KVROLE ? 4 : 2;
  for (int s = 0; s < 64; ++s) {
    const unsigned buf = (s & 1) * 36864u;
    if (s == 0) WAIT_VM(0); else if (NST == 4) WAIT_VM(4); else WAIT_VM(2);
    bar();
    if (s >= 2 && wave == 0 && lane == 0) __hip_atomic_store(cnt, (unsigned)(s - 1), RLX_AGENT);
    const int sn = s + 1 < 64 ? s + 1 : 63;
    f32x4 aA[2][2], aB[2][2];
#pragma unroll
    for (int T = 0; T < 2; ++T)
#pragma unroll
      for (int t = 0; t < 2; ++t) { aA[T][t] = f32x4{0, 0, 0, 0}; aB[T][t] = f32x4{0, 0, 0, 0}; }
    constexpr int PD = KVROLE ? 2 : 3;
    u32x4 xf[PD + 1][2];
#pragma unroll
    for (int k = 0; k < PD; ++k) { xf[k][0] = xs.frag(buf, 0, k); xf[k][1] = xs.frag(buf, 1, k); }
#pragma unroll
    for (int ks = 0; ks < 18; ++ks) {
      if (ks + PD < 18) { xf[(ks + PD) % (PD + 1)][0] = xs.frag(buf, 0, ks + PD); xf[(ks + PD) % (PD + 1)][1] = xs.frag(buf, 1, ks + PD); }
      if ((ks & 1) == 0) xs.issue1(rx, sn, buf ^ 36864u, wave, ks >> 1);
      __builtin_amdgcn_sched_barrier(0);
      const u32x4 x0 = xf[ks % (PD + 1)][0], x1 = xf[ks % (PD + 1)][1];
#pragma unroll
      for (int t = 0; t < 2; ++t) {
        aA[0][t] = mfma32(wa[t][ks], x0, aA[0][t]); aA[1][t] = mfma32(wa[t][ks], x1, aA[1][t]);
        if (KVROLE) { aB[0][t] = mfma32(x0, wb[t][ks], aB[0][t]); aB[1][t] = mfma32(x1, wb[t][ks], aB[1][t]); }
      }
      __builtin_amdgcn_sched_barrier(0);
    }
    MFMA_DONE();
    const unsigned o = KV + (unsigned)((b * 64 + s) * 32 + h) * 24576u;
#pragma unroll
    for (int T = 0; T < 2; ++T)
      st128(u32x4{pk_bf16(aA[T][0][0], aA[T][0][1]), pk_bf16(aA[T][0][2], aA[T][0][3]), pk_bf16(aA[T][1][0], aA[T][1][1]), pk_bf16(aA[T][1][2], aA[T][1][3])}, rws, l16, o + (KVROLE ? 0 : 16384) + (T * 4 + wave) * 1024);
    if (KVROLE) {
#pragma unroll
      for (int t = 0; t < 2; ++t)
        st128(u32x4{pk_bf16(aB[0][t][0], aB[0][t][1]), pk_bf16(aB[0][t][2], aB[0][t][3]), pk_bf16(aB[1][t][0], aB[1][t][1]), pk_bf16(aB[1][t][2], aB[1][t][3])}, rws, l16, o + 8192 + (2 * wave + t) * 1024);
    }
    if (KVROLE && s < 2) {
      WAIT_VM(0); bar();
      if (wave == 0 && lane == 0) __hip_atomic_store(cnt, (unsigned)(s + 1), RLX_AGENT);
      if (s == 0) TLS(prm, 2);
    }
  }
  WAIT_VM(0); bar();
  if (wave == 0 && lane == 0) __hip_atomic_store(cnt, 64u, RLX_AGENT);
  TLS(prm, 3);
}

DEV void swap16(float v, float& a, float& b) { a = v; b = v; asm volatile("s_nop 1\n\tv_permlane16_swap_b32 %0, %1" : "+v"(a), "+v"(b)); }
DEV void swap32(float v, float& a, float& b) { a = v; b = v; asm volatile("s_nop 1\n\tv_permlane32_swap_b32 %0, %1" : "+v"(a), "+v"(b)); }
DEV float xmax16(float v) {
  float a, b; swap16(v, a, b); v = fmaxf(a, b); swap32(v, a, b); return fmaxf(a, b);
}
DEV float xsum16(float v) { float a, b; swap16(v, a, b); v = a + b; swap32(v, a, b); return a + b; }
DEV unsigned ctl_load_async(__amdgpu_buffer_rsrc_t r, unsigned off) {
  unsigned v;
  asm volatile("buffer_load_dword %0, off, %1, %2 sc1" : "=v"(v) : "s"(r), "s"(off) : "memory");
  return v;
}

DEV void role_p2(const Params& prm, int b, int h, int wave, int lane, int tl_id) {
  gu32* ctl = (gu32*)prm.p[10];
  const __amdgpu_buffer_rsrc_t rws = mkrsrc(prm.p[10], WS_BYTES), rnull = mkrsrc(prm.p[10], 0);
  constexpr unsigned KVB = 0, KMB = 49152, BC = 57344;
  const int i16 = lane & 15, g = lane >> 4, T = wave >> 1, half = wave & 1;
  const unsigned l16 = lane * 16;
  const unsigned trq = 64 * g + 16 * ((lane >> 2) & 3) + 8 * (lane & 1);
  const float scale = 0.08838834764831845f;
  const unsigned kmc_off = KMC_OFF + (b * 32 + h) * 128, kvc_off = KVC_OFF + (b * 32 + h) * 256, ocs = b * 2 + (h >> 4);
  const unsigned kv0 = KV + (unsigned)(b * 64 * 32 + h) * 24576u, km0 = KVM + (unsigned)(b * 64 * 32 + h) * 4096u;
  unsigned km_seen = 0, kv_seen = 0;
  bool haveKV = false, haveKM = false;
  bool pending = false;
  TLS(prm, 4);
  for (int s = 0; s < 64; ++s) {
    if (s == 8) TLS(prm, 5); if (s == 16) TLS(prm, 6); if (s == 32) TLS(prm, 7); if (s == 48) TLS(prm, 8); if (s == 63) TLS(prm, 9); if (s == 62) TLS(prm, 12);
    const unsigned par = s & 1, kvb = KVB + par * 24576u, kmb = KMB + par * 4096u;
    TLX(prm, 0);
    if (!haveKM && pending) {
      WAIT_VM(0); bar();
      if (wave == 0 && lane == 0) __hip_atomic_fetch_add(ctl + (OC_OFF + ((s - 1) * 4 + ocs) * 128) / 4, 1u, RLX_AGENT);
      pending = false;
    }
    if (!haveKV) {
      if ((unsigned)s >= kv_seen) {
        if (wave == 0 && lane == 0) {
          wait_ge(ctl, kvc_off, (unsigned)s + 1); wait_ge(ctl, kvc_off + 128, (unsigned)s + 1);
          const unsigned c0 = __hip_atomic_load(ctl + kvc_off / 4, RLX_AGENT), c1 = __hip_atomic_load(ctl + (kvc_off + 128) / 4, RLX_AGENT);
          *(LAS unsigned*)BC = c0 < c1 ? c0 : c1;
        }
        bar();
        kv_seen = __builtin_amdgcn_readfirstlane(*(LAS unsigned*)BC);
        if (kv_seen <= (unsigned)s) kv_seen = s + 1;
      }
#pragma unroll
      for (int i = 0; i < 6; ++i) dma16_sc1(rws, l16, kv0 + (unsigned)s * (32u * 24576u) + (wave * 6 + i) * 1024u, kvb + (wave * 6 + i) * 1024u);
    }
    if (!haveKV) WAIT_VM(0); else WAIT_VM(2);
    bar();
    TLX(prm, 1);
    unsigned kmv = ctl_load_async(rws, kmc_off), kvv = ctl_load_async(rws, kvc_off), qv = ctl_load_async(rws, kvc_off + 128);
    const bool nKV = s + 1 < 64 && (unsigned)s + 1 < kv_seen;
    bool nKM = false;
    if (s < 63) {
      const __amdgpu_buffer_rsrc_t rkv = nKV ? rws : rnull;
      const unsigned kvn = kv0 + (unsigned)(s + 1 < 64 ? s + 1 : 63) * (32u * 24576u) + wave * 6144u, kvl = KVB + (par ^ 1) * 24576u + wave * 6144u;
#pragma unroll
      for (int i = 0; i < 6; ++i) dma16_sc1(rkv, l16, kvn + i * 1024u, kvl + i * 1024u);
    }
    u32x4 qf[4];
#pragma unroll
    for (int kp = 0; kp < 4; ++kp) qf[kp] = lds_rd128(kvb + 16384 + (T * 4 + kp) * 1024 + l16);
    f32x4 s1 = {0, 0, 0, 0}, s2 = {0, 0, 0, 0}, sm = {0, 0, 0, 0};
#pragma unroll
    for (int kp = 0; kp < 4; ++kp) { s1 = mfma32(lds_rd128(kvb + kp * 1024 + l16), qf[kp], s1); s2 = mfma32(lds_rd128(kvb + 4096 + kp * 1024 + l16), qf[kp], s2); }
    MFMA_DONE();
    if (!haveKM) {
      if ((unsigned)s >= km_seen) {
        if (wave == 0 && lane == 0) { wait_ge(ctl, kmc_off, (unsigned)(s + 1)); *(LAS unsigned*)(BC + 4) = __hip_atomic_load(ctl + kmc_off / 4, RLX_AGENT); }
        bar();
        const unsigned v = __builtin_amdgcn_readfirstlane(*(LAS unsigned*)(BC + 4));
        km_seen = v > km_seen ? v : km_seen;
      }
      if (s == 62) TLS(prm, 13); if (s == 63) TLS(prm, 11);
      dma16_sc1(rws, l16, km0 + (unsigned)s * (32u * 4096u) + wave * 1024u, kmb + wave * 1024u);
      WAIT_VM(0);
      bar();
      if (s == 63) TLS(prm, 14);
    }
    if (s < 63) {
      const bool nkm = (unsigned)s + 1 < km_seen;
      nKM = nkm;
      dma16_sc1(nkm ? rws : rnull, l16, km0 + (unsigned)(s + 1 < 64 ? s + 1 : 63) * (32u * 4096u) + wave * 1024u, KMB + (par ^ 1) * 4096u + wave * 1024u);
    }
    TLX(prm, 2);
#pragma unroll
    for (int kp = 0; kp < 4; ++kp) {
      const u32x2 lo = tr_read(kmb + trq + 512 * kp), hi = tr_read(kmb + trq + 512 * kp + 256);
      sm = mfma32(u32x4{lo.x, lo.y, hi.x, hi.y}, qf[kp], sm);
    }
    MFMA_DONE();
    u32x4 vt[4];
    u32x2 vm[4];
#pragma unroll
    for (int e = 0; e < 4; ++e) { vt[e] = lds_rd128(kvb + 8192 + (half * 4 + e) * 1024 + l16); vm[e] = *(LAS u32x2*)(kmb + 2048 + (16 * (half * 4 + e) + i16) * 16 + (g & 1) * 8); }
    asm volatile("" :: "v"(sm), "v"(s1), "v"(s2)); TLX(prm, 4);
    float sc[12];
#pragma unroll
    for (int r = 0; r < 4; ++r) {
      sc[r] = g < 2 ? rbf(rbf(sm[r]) * scale) : -1e9f;
      sc[4 + r] = (T == 1 || 4 * g + r <= i16) ? rbf(rbf(s1[r]) * scale) : -1e9f;
      sc[8 + r] = (T == 1 && 4 * g + r <= i16) ? rbf(rbf(s2[r]) * scale) : -1e9f;
    }
    float mx = sc[0];
#pragma unroll
    for (int r = 1; r < 12; ++r) mx = fmaxf(mx, sc[r]);
    mx = xmax16(mx);
    float sum = 0.f;
#pragma unroll
    for (int r = 0; r < 12; ++r) { sc[r] = __builtin_amdgcn_exp2f((sc[r] - mx) * 1.4426950408889634f); sum += sc[r]; }
    sum = xsum16(sum);
    const float inv = __builtin_amdgcn_rcpf(sum);
    const u32x2 pm = {pk_bf16(sc[0] * inv, sc[1] * inv), pk_bf16(sc[2] * inv, sc[3] * inv)};
    const u32x4 pt = {pk_bf16(sc[4] * inv, sc[5] * inv), pk_bf16(sc[6] * inv, sc[7] * inv), pk_bf16(sc[8] * inv, sc[9] * inv), pk_bf16(sc[10] * inv, sc[11] * inv)};
    asm volatile("" :: "v"(pm), "v"(pt)); TLX(prm, 5);
    f32x4 o[4];
#pragma unroll
    for (int e = 0; e < 4; ++e) o[e] = mfma32(vt[e], pt, f32x4{0, 0, 0, 0});
    MFMA_DONE();
#pragma unroll
    for (int e = 0; e < 4; ++e) o[e] = mfma16(g < 2 ? vm[e] : u32x2{0u, 0u}, pm, o[e]);
    MFMA_DONE();
    const unsigned oo = OB + (unsigned)((b * 64 + s) * 32 + h) * 8192u;
#pragma unroll
    for (int k2 = 0; k2 < 2; ++k2) {
      const u32x4 v = {pk_bf16(o[2 * k2][0], o[2 * k2][1]), pk_bf16(o[2 * k2][2], o[2 * k2][3]), pk_bf16(o[2 * k2 + 1][0], o[2 * k2 + 1][1]), pk_bf16(o[2 * k2 + 1][2], o[2 * k2 + 1][3])};
      st128(v, rws, l16 + k2 * 1024, oo + (T * 4 + half * 2) * 1024);
    }
    TLX(prm, 6);
    if (s == 63) {
      WAIT_VM(0);
      asm volatile("" : "+v"(kmv), "+v"(kvv), "+v"(qv));
      bar();
      if (wave == 0 && lane == 0) {
        if (pending) __hip_atomic_fetch_add(ctl + (OC_OFF + (62 * 4 + ocs) * 128) / 4, 1u, RLX_AGENT);
        __hip_atomic_fetch_add(ctl + (OC_OFF + (63 * 4 + ocs) * 128) / 4, 1u, RLX_AGENT);
      }
      break;
    }
    WAIT_VM(9);
    asm volatile("" : "+v"(kmv), "+v"(kvv), "+v"(qv));
    if (wave == 0 && lane == 0) { *(LAS unsigned*)(BC + 8) = kmv; *(LAS unsigned*)(BC + 12) = kvv < qv ? kvv : qv; }
    bar();
    if (pending && wave == 0 && lane == 0) __hip_atomic_fetch_add(ctl + (OC_OFF + ((s - 1) * 4 + ocs) * 128) / 4, 1u, RLX_AGENT);
    {
      const unsigned a = __builtin_amdgcn_readfirstlane(*(LAS unsigned*)(BC + 8)), c = __builtin_amdgcn_readfirstlane(*(LAS unsigned*)(BC + 12));
      km_seen = a > km_seen ? a : km_seen;
      kv_seen = c > kv_seen ? c : kv_seen;
    }
    haveKV = nKV; haveKM = nKM; pending = true;
  }
  TLS(prm, 10);
}

DEV void role_p3(const Params& prm, int slice, int T, int wave, int lane, int tl_id) {
  gu32* ctl = (gu32*)prm.p[10];
  const __amdgpu_buffer_rsrc_t rws = mkrsrc(prm.p[10], WS_BYTES);
  const int i16 = lane & 15, g = lane >> 4, q = (lane >> 2) & 3, p = lane & 3;
  const unsigned l16 = lane * 16, RW = 16384u + (unsigned)wave * 36864u;
  u32x4 w0[2][8][4], w1[7][4];
  const unsigned trb = (4 * g + q) * 64 + 8 * p;
  if (wave == 0 && lane == 0) wait_ge(ctl, WOCC_OFF, 64u);
  bar();
  auto stage = [&](int b, int row0, int n16, unsigned dst) {
    unsigned so = WOC + (unsigned)(slice * 64 + b * 32) * 8192u + (unsigned)(wave * 1024 + row0) * 64u, ld = dst;
#pragma unroll 1
    for (int i = 0; i < n16; ++i) { dma16_sc1(rws, l16, so, ld); so += 1024u; ld += 1024u; }
    WAIT_VM(0);
  };
  auto rd = [&](unsigned base, int hl, int kp, int t) {
    const unsigned a = base + trb + (hl * 128 + kp * 32) * 64 + t * 32;
    const u32x2 lo = tr_read(a), hi = tr_read(a + 1024);
    return u32x4{lo.x, lo.y, hi.x, hi.y};
  };
  u32x4 hold[4];
#pragma unroll
  for (int hf = 0; hf < 2; ++hf) {
    stage(0, hf * 512, 32, RW);
#pragma unroll
    for (int hl = 0; hl < 4; ++hl)
#pragma unroll
      for (int kp = 0; kp < 4; ++kp) {
        w0[0][hf * 4 + hl][kp] = rd(RW, hl, kp, 0);
        if (hf * 4 + hl < 7) w1[hf * 4 + hl][kp] = rd(RW, hl, kp, 1); else hold[kp] = rd(RW, hl, kp, 1);
      }
    WAIT_LGKM0();
  }
#pragma unroll
  for (int kp = 0; kp < 4; ++kp) lds_wr128(RW + 32768 + kp * 1024 + l16, hold[kp]);
  {
    stage(1, 0, 32, RW);
    u32x4 tmp[4][4];
#pragma unroll
    for (int hl = 0; hl < 4; ++hl)
#pragma unroll
      for (int kp = 0; kp < 4; ++kp) { w0[1][hl][kp] = rd(RW, hl, kp, 0); tmp[hl][kp] = rd(RW, hl, kp, 1); }
    WAIT_LGKM0();
#pragma unroll
    for (int hl = 0; hl < 4; ++hl)
#pragma unroll
      for (int kp = 0; kp < 4; ++kp) lds_wr128(RW + (hl * 4 + kp) * 1024 + l16, tmp[hl][kp]);
#pragma unroll
    for (int hf = 0; hf < 2; ++hf) {
      stage(1, 512 + hf * 256, 16, RW + 16384);
#pragma unroll
      for (int hl = 0; hl < 2; ++hl)
#pragma unroll
        for (int kp = 0; kp < 4; ++kp) { w0[1][4 + hf * 2 + hl][kp] = rd(RW + 16384, hl, kp, 0); tmp[hf * 2 + hl][kp] = rd(RW + 16384, hl, kp, 1); }
      WAIT_LGKM0();
    }
#pragma unroll
    for (int hl = 0; hl < 4; ++hl)
#pragma unroll
      for (int kp = 0; kp < 4; ++kp) lds_wr128(RW + 16384 + (hl * 4 + kp) * 1024 + l16, tmp[hl][kp]);
  }
  TLS(prm, 4);
  if (wave == 0 && lane == 0) wait_ge(ctl, GATEC_OFF, 64u);
  bar();
  TLS(prm, 5);
  const float* gate = (const float*)((const char*)prm.p[10] + GATE);
  constexpr int W = 22;
  int ntask = 0; (void)ntask;
  for (;;) {
    if (wave == 0) {
      unsigned tk = 0;
      if (lane == 0) tk = __hip_atomic_fetch_add(ctl + (TQ_OFF + (slice * 2 + T) * 128) / 4, 1u, RLX_AGENT);
      tk = __builtin_amdgcn_readfirstlane(tk);
      if (tk < 64 && lane < 4) wait_ge(ctl, OC_OFF + (tk * 4 + lane) * 128, 16u);
      if (lane == 0) *(LAS unsigned*)16 = tk;
    }
    bar();
    const int s = __builtin_amdgcn_readfirstlane(*(LAS unsigned*)16);
    if (s >= 64) break;
    if (ntask == 2) TLS(prm, 6); if (ntask == 4) TLS(prm, 7); if (s == 63) TLS(prm, 11);
    ++ntask;
    const unsigned oo = OB + (unsigned)(s * 32 + wave * 8) * 8192u + T * 4096;
    u32x4 of[W];
#pragma unroll
    for (int n = 0; n < W; ++n) of[n] = __builtin_amdgcn_raw_buffer_load_b128(rws, l16 + (n & 3) * 1024, oo + (n >> 5) * (64u * 32u * 8192u) + ((n >> 2) & 7) * 8192u, 16);
    f32x4 acc[2][2];
#pragma unroll
    for (int b = 0; b < 2; ++b) { acc[b][0] = f32x4{0, 0, 0, 0}; acc[b][1] = f32x4{0, 0, 0, 0}; }
    u32x4 wl[3];
#pragma unroll
    for (int n = 0; n < 64; ++n) {
      const int b = n >> 5, hh = (n >> 2) & 7, kp = n & 3;
      if (n + 2 >= 28 && n + 2 < 64) wl[(n + 2) % 3] = lds_rd128(RW + (n + 2 < 32 ? 32 + (n + 2 - 28) : n + 2 - 32) * 1024 + l16);
      __builtin_amdgcn_sched_barrier(0);
      acc[b][0] = mfma32(w0[b][hh][kp], of[n % W], acc[b][0]);
      acc[b][1] = mfma32(n < 28 ? w1[hh < 7 ? hh : 0][kp] : wl[n % 3], of[n % W], acc[b][1]);
      __builtin_amdgcn_sched_barrier(0);
      if (n + W < 64) { const int m = n + W; of[n % W] = __builtin_amdgcn_raw_buffer_load_b128(rws, l16 + (m & 3) * 1024, oo + (m >> 5) * (64u * 32u * 8192u) + ((m >> 2) & 7) * 8192u, 16); }
    }
    MFMA_DONE();
#pragma unroll
    for (int b = 0; b < 2; ++b)
#pragma unroll
      for (int t = 0; t < 2; ++t) *(LAS f32x4*)(wave * 4096 + (b * 2 + t) * 1024 + l16) = acc[b][t];
    bar();
    if (wave < 2) {
      f32x4 a = {0, 0, 0, 0}, bb = {0, 0, 0, 0};
#pragma unroll
      for (int w = 0; w < 4; ++w) { a += *(LAS f32x4*)(w * 4096 + wave * 1024 + l16); bb += *(LAS f32x4*)(w * 4096 + (2 + wave) * 1024 + l16); }
      const int tok = s * 32 + T * 16 + i16;
      const float gt = __builtin_bit_cast(float, __hip_atomic_load((gu32*)gate + tok, RLX_AGENT)), og = rbf(1.0f - gt);
      float r[4];
#pragma unroll
      for (int k = 0; k < 4; ++k) r[k] = rbf(gt * rbf(a[k])) + rbf(og * rbf(bb[k]));
      *(GAS u32x2*)((GAS char*)prm.p[13] + (size_t)tok * 1152 + slice * 64 + wave * 32 + g * 8) = u32x2{pk_bf16(r[0], r[1]), pk_bf16(r[2], r[3])};
    }
  }
  TLS(prm, 10);
}

DEV void role_regroup(const Params& prm, int b, int h, int wave, int lane) {
  gu32* ctl = (gu32*)prm.p[10];
  const __amdgpu_buffer_rsrc_t rws = mkrsrc(prm.p[10], WS_BYTES), rw = mkrsrc((const char*)prm.p[b * 5 + 3] + (size_t)h * 147456, 147456u);
  unsigned so = (unsigned)wave * 36864u;
#pragma unroll 1
  for (int i = 0; i < 36; ++i) { dma16(rw, lane * 16, so, so); so += 1024u; }
  WAIT_VM(0); bar();
  const unsigned src = (wave * 32 + (lane >> 2)) * 1152 + (lane & 3) * 16, dst = WOC + (unsigned)(b * 32 + h) * 8192u + wave * 2048;
#pragma unroll 2
  for (int sl = 0; sl < 18; ++sl) {
    st128(lds_rd128(src + sl * 64), rws, lane * 16, dst + sl * (64u * 8192u));
    st128(lds_rd128(src + sl * 64 + 16 * 1152), rws, lane * 16, dst + sl * (64u * 8192u) + 1024);
  }
  WAIT_VM(0); bar();
  if (wave == 0 && lane == 0) __hip_atomic_fetch_add(ctl + WOCC_OFF / 4, 1u, RLX_AGENT);
}

DEV void role_gate(const Params& prm, int idx, int wave, int lane) {
  gu32* ctl = (gu32*)prm.p[10];
  const unsigned short* x = (const unsigned short*)prm.p[11]; const unsigned short* gw = (const unsigned short*)prm.p[12];
  float w[9];
#pragma unroll
  for (int k = 0; k < 9; ++k) w[k] = __builtin_bit_cast(float, (unsigned)gw[lane + 64 * k] << 16);
  for (int tok = idx * 4 + wave; tok < 2048; tok += 256) {
    float s = 0.f;
#pragma unroll
    for (int k = 0; k < 9; ++k) s += rbf(__builtin_bit_cast(float, (unsigned)x[tok * 576 + lane + 64 * k] << 16) * w[k]);
#pragma unroll
    for (int m = 32; m >= 1; m >>= 1) s += __shfl_xor(s, m);
    const float z = rbf(s);
    const float gt = rbf(1.0f / (1.0f + __expf(-z)));
    if (lane == 0) __hip_atomic_store((gu32*)((char*)prm.p[10] + GATE) + tok, __builtin_bit_cast(unsigned, gt), RLX_AGENT);
  }
  WAIT_VM(0); bar();
  if (wave == 0 && lane == 0) __hip_atomic_fetch_add(ctl + GATEC_OFF / 4, 1u, RLX_AGENT);
}

DEV void probe_polarity(const Params& prm, int b, int h, int lane) {
  gu32* ctl = (gu32*)prm.p[10];
  const uintptr_t wsb = (uintptr_t)prm.p[10];
  const unsigned xo = (unsigned)(((wsb + XMIN + 0x1fffff) & ~(uintptr_t)0x1fffff) - wsb) + ((unsigned)b << 21) + (116u + (h >> 3)) * 16384u + (h & 7) * 1024u;
  gu32* base = (gu32*)((char*)prm.p[10] + xo);
  if (lane < 8) __hip_atomic_store(base + (lane >> 2) * 2048 + (lane & 3) * 64, 0u, RLX_AGENT);
  WAIT_VM(0);
  unsigned t[2] = {~0u, ~0u};
#pragma unroll 1
  for (int k = 0; k < 4; ++k) {
#pragma unroll
    for (int c = 0; c < 2; ++c) {
      const unsigned long long t0 = __builtin_amdgcn_s_memtime();
      const unsigned v = __hip_atomic_load(base + c * 2048 + k * 64, RLX_AGENT);
      asm volatile("s_waitcnt vmcnt(0)" :: "v"(v) : "memory");
      const unsigned d = (unsigned)(__builtin_amdgcn_s_memtime() - t0);
      t[c] = d < t[c] ? d : t[c];
    }
  }
  if (lane == 0) {
    gu32* w = ctl + (POL_OFF + b * 64) / 4;
    __hip_atomic_fetch_max(w + 1, ~t[0], RLX_AGENT); __hip_atomic_fetch_max(w + 2, ~t[1], RLX_AGENT);
    WAIT_VM(0);
    if (__hip_atomic_fetch_add(w + 3, 1u, RLX_AGENT) == 31u) {
      const unsigned m0 = ~__hip_atomic_load(w + 1, RLX_AGENT), m1 = ~__hip_atomic_load(w + 2, RLX_AGENT);
      __hip_atomic_store(w, 1u + (m1 < m0 ? 1u : 0u), RLX_AGENT);
    }
  }
}

__global__ void __launch_bounds__(256, 1) gated_mix_kernel(Params prm) {
  extern __shared__ __attribute__((aligned(16))) unsigned char lds[];
  const int lane = threadIdx.x & 63;
  const int wave = __builtin_amdgcn_readfirstlane(threadIdx.x >> 6);
  if (threadIdx.x == 0) {
    const unsigned half = ((unsigned)__builtin_amdgcn_s_getreg(20 | (3 << 11)) >> 2) & 1u;
    const unsigned t = __hip_atomic_fetch_add((gu32*)prm.p[10] + TICK_OFF / 4 + half * 16, 1u, RLX_AGENT);
    *(LAS unsigned*)16 = t < 128 ? (half << 7 | t) : ((half ^ 1u) << 7 | (255u - t));
  }
  bar();
  const int role = __builtin_amdgcn_readfirstlane(*(LAS unsigned*)16);
  bar();
  const int b = role >> 7, t = role & 127;
  const int tl_id = role; (void)tl_id;
  TLS(prm, 0);
  if (t < 32) {
    asm volatile(
      "s_mov_b64 s[64:65], %0\n"
      "s_mov_b32 s58, %1\n"
      "s_mov_b32 s34, %2\n"
      "s_mov_b32 s57, %3\n"
      "s_load_dwordx2 s[80:81], s[64:65], 0x50\n"
      "s_mul_i32 s44, s58, 40\n"
      "s_add_u32 s64, s64, s44\n"
      "s_addc_u32 s65, s65, 0\n"
      "s_load_dwordx2 s[70:71], s[64:65], 0x0\n"
      "s_load_dwordx2 s[72:73], s[64:65], 0x8\n"
      "s_load_dwordx2 s[74:75], s[64:65], 0x10\n"
      "s_load_dwordx2 s[76:77], s[64:65], 0x18\n"
      "s_load_dwordx2 s[78:79], s[64:65], 0x20\n"
      "s_waitcnt lgkmcnt(0)\n"
      "s_mov_b64 exec, -1\n"
      "s_mov_b32 s24, s80\n"
      "s_and_b32 s25, s81, 0xffff\n"
      "s_mov_b32 s26, 169869312\n"
      "s_mov_b32 s27, 0x20000\n"
      "s_mov_b32 s42, 0x200000\n"
      "v_mbcnt_lo_u32_b32 v16, -1, 0\n"
      "v_mbcnt_hi_u32_b32 v16, -1, v16\n"
      "v_lshlrev_b32 v0, 4, v16\n"
      "v_and_b32 v17, 15, v16\n"
      "v_lshrrev_b32 v18, 4, v16\n"
      "v_bfe_u32 v19, v16, 2, 2\n"
      "v_and_b32 v20, 3, v16\n"
      "v_and_b32 v21, 8, v16\n"
      "v_mov_b32 v1, 0x4444\n"
      "v_mov_b32 v22, 0xeeee\n"
      "v_cmp_ne_u32 vcc, 0, v21\n"
      "s_nop 1\n"
      "v_cndmask_b32 v1, v1, v22, vcc\n"
      "v_and_b32 v22, 1, v20\n"
      "v_lshlrev_b32 v22, 3, v22\n"
      "v_lshl_add_u32 v22, v19, 4, v22\n"
      "v_lshl_add_u32 v4, v18, 6, v22\n"
      "v_and_b32 v22, 7, v16\n"
      "v_mul_u32_u24 v22, 1168, v22\n"
      "v_lshl_add_u32 v2, v18, 4, v22\n"
      "s_lshl_b32 s44, s57, 9\n"
      "v_lshrrev_b32 v21, 5, v16\n"
      "v_lshlrev_b32 v21, 8, v21\n"
      "v_lshl_add_u32 v21, v17, 4, v21\n"
      "v_and_b32 v22, 1, v18\n"
      "v_lshl_add_u32 v21, v22, 3, v21\n"
      "v_add_u32 v3, s44, v21\n"
      "s_lshl_b32 s44, s57, 12\n"
      "s_add_u32 s44, s44, 24576\n"
      "v_add_u32 v5, s44, v0\n"
      "v_bfe_u32 v21, v16, 1, 3\n"
      "v_mul_u32_u24 v21, 1168, v21\n"
      "v_lshl_add_u32 v21, v18, 5, v21\n"
      "v_and_b32 v22, 1, v16\n"
      "v_lshl_add_u32 v21, v22, 4, v21\n"
      "s_lshl_b32 s44, s57, 7\n"
      "v_add_u32 v6, s44, v21\n"
      "s_add_u32 s44, s80, 3145727\n"
      "s_and_b32 s44, s44, 0xffe00000\n"
      "s_sub_u32 s44, s44, s80\n"
      "s_lshl_b32 s45, s58, 21\n"
      "s_add_u32 s36, s44, s45\n"
      "s_lshl_b32 s44, s34, 14\n"
      "s_lshl_b32 s45, s57, 11\n"
      "s_add_u32 s37, s44, s45\n"
      "s_lshr_b32 s44, s34, 3\n"
      "s_add_u32 s44, s44, 32\n"
      "s_lshl_b32 s44, s44, 14\n"
      "s_and_b32 s45, s34, 7\n"
      "s_lshl_b32 s45, s45, 10\n"
      "s_add_u32 s44, s44, s45\n"
      "s_lshl_b32 s45, s57, 8\n"
      "s_add_u32 s38, s44, s45\n"
      "s_mov_b32 s43, -1\n"
      "s_cmp_eq_u32 s57, 3\n"
      "s_cselect_b32 s43, s34, s43\n"
      "s_add_u32 s45, s34, 32\n"
      "s_cmp_lt_u32 s34, 4\n"
      "s_cselect_b32 s45, s45, -1\n"
      "s_cmp_eq_u32 s57, 2\n"
      "s_cselect_b32 s43, s45, s43\n"
      "s_lshl_b32 s44, s34, 8\n"
      "s_add_u32 s45, s44, 524288\n"
      "s_cmp_eq_u32 s57, 3\n"
      "s_cselect_b32 s56, s44, s45\n"
      "s_mov_b32 s59, 1024\n"
      "s_mov_b32 s60, 4096\n"
      "s_mov_b32 s68, 12288\n"
      "s_cselect_b32 s59, 16384, s59\n"
      "s_cselect_b32 s60, 65536, s60\n"
      "s_cselect_b32 s68, 65536, s68\n"
      "s_add_u32 s45, s44, 16384\n"
      "s_cmp_eq_u32 s57, 3\n"
      "s_cselect_b32 s66, s44, s45\n"
      "s_add_u32 s66, s66, 1769472\n"
      "s_lshl_b32 s44, s58, 11\n"
      "s_add_u32 s44, s44, s34\n"
      "s_mul_i32 s45, s44, 24576\n"
      "s_add_u32 s39, s45, 25165824\n"
      "s_mul_i32 s45, s44, 4096\n"
      "s_add_u32 s40, s45, 8388608\n"
      "s_lshl_b32 s44, s58, 5\n"
      "s_add_u32 s44, s44, s34\n"
      "s_lshl_b32 s45, s44, 7\n"
      "s_add_u32 s54, s45, 32768\n"
      "s_mov_b32 s55, 0x3e0293ee\n"
      "s_mov_b32 s96, s55\n"
      "s_mov_b32 s97, s55\n"
      "v_lshrrev_b32 v21, 1, v17\n"
      "v_and_b32 v22, 1, v18\n"
      "v_lshlrev_b32 v22, 1, v22\n"
      "v_xor_b32 v22, v21, v22\n"
      "v_xor_b32 v23, 4, v22\n"
      "v_and_b32 v24, 1, v17\n"
      "v_lshlrev_b32 v24, 4, v24\n"
      "v_lshl_add_u32 v24, v18, 13, v24\n"
      "v_lshl_add_u32 v28, v22, 5, v24\n"
      "v_lshl_add_u32 v29, v23, 5, v24\n"
      "v_lshrrev_b32 v21, 1, v18\n"
      "v_lshlrev_b32 v21, 3, v21\n"
      "v_and_b32 v22, 1, v18\n"
      "v_lshl_add_u32 v21, v22, 2, v21\n"
      "v_and_b32 v23, 1, v19\n"
      "v_add_u32 v21, v21, v23\n"
      "v_lshlrev_b32 v21, 8, v21\n"
      "v_lshl_add_u32 v21, v20, 3, v21\n"
      "v_lshlrev_b32 v22, 2, v22\n"
      "v_lshl_or_b32 v22, v23, 1, v22\n"
      "v_lshrrev_b32 v23, 1, v19\n"
      "s_lshl_b32 s44, s57, 1\n"
      "v_add_u32 v23, s44, v23\n"
      "v_xor_b32 v23, v23, v22\n"
      "v_lshl_add_u32 v32, v23, 5, v21\n"
      "v_and_b32 v21, 1, v18\n"
      "v_lshlrev_b32 v21, 4, v21\n"
      "v_lshrrev_b32 v22, 1, v18\n"
      "v_lshl_add_u32 v21, v22, 2, v21\n"
      "v_and_b32 v22, 1, v19\n"
      "v_add_u32 v21, v21, v22\n"
      "v_lshrrev_b32 v22, 1, v19\n"
      "v_lshl_add_u32 v23, v22, 5, v21\n"
      "v_mul_u32_u24 v21, 0x480, v21\n"
      "v_lshl_add_u32 v21, v20, 3, v21\n"
      "v_lshl_add_u32 v21, v22, 6, v21\n"
      "s_lshl_b32 s44, s57, 8\n"
      "v_add_u32 v34, s44, v21\n"
      "v_mul_u32_u24 v23, 0x480, v23\n"
      "v_lshl_add_u32 v23, v20, 3, v23\n"
      "s_lshl_b32 s44, s57, 5\n"
      "s_add_u32 s44, s44, 1024\n"
      "v_add_u32 v36, s44, v23\n"
      "s_mul_i32 s44, s34, 256\n"
      "s_add_u32 s28, s70, s44\n"
      "s_addc_u32 s29, s71, 0\n"
      "s_and_b32 s29, s29, 0xffff\n"
      "s_mov_b32 s30, 4710656\n"
      "s_mov_b32 s31, 0x20000\n"
      "s_mul_i32 s44, s57, 393216\n"
      "s_add_u32 s44, s44, 0\n"
      "s_mul_i32 s45, s57, 12288\n"
      "s_add_u32 m0, s45, 0\n"
      "s_nop 0\n"
      "buffer_load_dwordx4 v28, s[28:31], s44 offen lds\n"
      "s_add_u32 s44, s44, 32768\n"
      "s_add_u32 m0, m0, 1024\n"
      "s_nop 0\n"
      "buffer_load_dwordx4 v29, s[28:31], s44 offen lds\n"
      "s_add_u32 s44, s44, 32768\n"
      "s_add_u32 m0, m0, 1024\n"
      "s_nop 0\n"
      "buffer_load_dwordx4 v28, s[28:31], s44 offen lds\n"
      "s_add_u32 s44, s44, 32768\n"
      "s_add_u32 m0, m0, 1024\n"
      "s_nop 0\n"
      "buffer_load_dwordx4 v29, s[28:31], s44 offen lds\n"
      "s_add_u32 s44, s44, 32768\n"
      "s_add_u32 m0, m0, 1024\n"
      "s_nop 0\n"
      "buffer_load_dwordx4 v28, s[28:31], s44 offen lds\n"
      "s_add_u32 s44, s44, 32768\n"
      "s_add_u32 m0, m0, 1024\n"
      "s_nop 0\n"
      "buffer_load_dwordx4 v29, s[28:31], s44 offen lds\n"
      "s_add_u32 s44, s44, 32768\n"
      "s_add_u32 m0, m0, 1024\n"
      "s_nop 0\n"
      "buffer_load_dwordx4 v28, s[28:31], s44 offen lds\n"
      "s_add_u32 s44, s44, 32768\n"
      "s_add_u32 m0, m0, 1024\n"
      "s_nop 0\n"
      "buffer_load_dwordx4 v29, s[28:31], s44 offen lds\n"
      "s_add_u32 s44, s44, 32768\n"
      "s_add_u32 m0, m0, 1024\n"
      "s_nop 0\n"
      "buffer_load_dwordx4 v28, s[28:31], s44 offen lds\n"
      "s_add_u32 s44, s44, 32768\n"
      "s_add_u32 m0, m0, 1024\n"
      "s_nop 0\n"
      "buffer_load_dwordx4 v29, s[28:31], s44 offen lds\n"
      "s_add_u32 s44, s44, 32768\n"
      "s_add_u32 m0, m0, 1024\n"
      "s_nop 0\n"
      "buffer_load_dwordx4 v28, s[28:31], s44 offen lds\n"
      "s_add_u32 s44, s44, 32768\n"
      "s_add_u32 m0, m0, 1024\n"
      "s_nop 0\n"
      "buffer_load_dwordx4 v29, s[28:31], s44 offen lds\n"
      "s_mul_i32 s44, s57, 393216\n"
      "s_add_u32 s44, s44, 1572864\n"
      "s_mul_i32 s45, s57, 12288\n"
      "s_add_u32 m0, s45, 49152\n"
      "s_nop 0\n"
      "buffer_load_dwordx4 v28, s[28:31], s44 offen lds\n"
      "s_add_u32 s44, s44, 32768\n"
      "s_add_u32 m0, m0, 1024\n"
      "s_nop 0\n"
      "buffer_load_dwordx4 v29, s[28:31], s44 offen lds\n"
      "s_add_u32 s44, s44, 32768\n"
      "s_add_u32 m0, m0, 1024\n"
      "s_nop 0\n"
      "buffer_load_dwordx4 v28, s[28:31], s44 offen lds\n"
      "s_add_u32 s44, s44, 32768\n"
      "s_add_u32 m0, m0, 1024\n"
      "s_nop 0\n"
      "buffer_load_dwordx4 v29, s[28:31], s44 offen lds\n"
      "s_add_u32 s44, s44, 32768\n"
      "s_add_u32 m0, m0, 1024\n"
      "s_nop 0\n"
      "buffer_load_dwordx4 v28, s[28:31], s44 offen lds\n"
      "s_add_u32 s44, s44, 32768\n"
      "s_add_u32 m0, m0, 1024\n"
      "s_nop 0\n"
      "buffer_load_dwordx4 v29, s[28:31], s44 offen lds\n"
      "s_add_u32 s44, s44, 32768\n"
      "s_add_u32 m0, m0, 1024\n"
      "s_nop 0\n"
      "buffer_load_dwordx4 v28, s[28:31], s44 offen lds\n"
      "s_add_u32 s44, s44, 32768\n"
      "s_add_u32 m0, m0, 1024\n"
      "s_nop 0\n"
      "buffer_load_dwordx4 v29, s[28:31], s44 offen lds\n"
      "s_add_u32 s44, s44, 32768\n"
      "s_add_u32 m0, m0, 1024\n"
      "s_nop 0\n"
      "buffer_load_dwordx4 v28, s[28:31], s44 offen lds\n"
      "s_add_u32 s44, s44, 32768\n"
      "s_add_u32 m0, m0, 1024\n"
      "s_nop 0\n"
      "buffer_load_dwordx4 v29, s[28:31], s44 offen lds\n"
      "s_add_u32 s44, s44, 32768\n"
      "s_add_u32 m0, m0, 1024\n"
      "s_nop 0\n"
      "buffer_load_dwordx4 v28, s[28:31], s44 offen lds\n"
      "s_add_u32 s44, s44, 32768\n"
      "s_add_u32 m0, m0, 1024\n"
      "s_nop 0\n"
      "buffer_load_dwordx4 v29, s[28:31], s44 offen lds\n"
      "s_mul_i32 s44, s57, 393216\n"
      "s_add_u32 s44, s44, 3145728\n"
      "s_mul_i32 s45, s57, 12288\n"
      "s_add_u32 m0, s45, 98304\n"
      "s_nop 0\n"
      "buffer_load_dwordx4 v28, s[28:31], s44 offen lds\n"
      "s_add_u32 s44, s44, 32768\n"
      "s_add_u32 m0, m0, 1024\n"
      "s_nop 0\n"
      "buffer_load_dwordx4 v29, s[28:31], s44 offen lds\n"
      "s_add_u32 s44, s44, 32768\n"
      "s_add_u32 m0, m0, 1024\n"
      "s_nop 0\n"
      "buffer_load_dwordx4 v28, s[28:31], s44 offen lds\n"
      "s_add_u32 s44, s44, 32768\n"
      "s_add_u32 m0, m0, 1024\n"
      "s_nop 0\n"
      "buffer_load_dwordx4 v29, s[28:31], s44 offen lds\n"
      "s_add_u32 s44, s44, 32768\n"
      "s_add_u32 m0, m0, 1024\n"
      "s_nop 0\n"
      "buffer_load_dwordx4 v28, s[28:31], s44 offen lds\n"
      "s_add_u32 s44, s44, 32768\n"
      "s_add_u32 m0, m0, 1024\n"
      "s_nop 0\n"
      "buffer_load_dwordx4 v29, s[28:31], s44 offen lds\n"
      "s_add_u32 s44, s44, 32768\n"
      "s_add_u32 m0, m0, 1024\n"
      "s_nop 0\n"
      "buffer_load_dwordx4 v28, s[28:31], s44 offen lds\n"
      "s_add_u32 s44, s44, 32768\n"
      "s_add_u32 m0, m0, 1024\n"
      "s_nop 0\n"
      "buffer_load_dwordx4 v29, s[28:31], s44 offen lds\n"
      "s_add_u32 s44, s44, 32768\n"
      "s_add_u32 m0, m0, 1024\n"
      "s_nop 0\n"
      "buffer_load_dwordx4 v28, s[28:31], s44 offen lds\n"
      "s_add_u32 s44, s44, 32768\n"
      "s_add_u32 m0, m0, 1024\n"
      "s_nop 0\n"
      "buffer_load_dwordx4 v29, s[28:31], s44 offen lds\n"
      "s_add_u32 s44, s44, 32768\n"
      "s_add_u32 m0, m0, 1024\n"
      "s_nop 0\n"
      "buffer_load_dwordx4 v28, s[28:31], s44 offen lds\n"
      "s_add_u32 s44, s44, 32768\n"
      "s_add_u32 m0, m0, 1024\n"
      "s_nop 0\n"
      "buffer_load_dwordx4 v29, s[28:31], s44 offen lds\n"
      "s_lshl_b32 s44, s57, 6\n"
      "v_add_u32 v38, s44, v16\n"
      "v_add_u32 v39, 0, v38\n"
      "v_min_u32 v39, 575, v39\n"
      "v_mul_u32_u24 v47, 911, v39\n"
      "v_lshrrev_b32 v47, 16, v47\n"
      "v_mul_u32_u24 v48, 72, v47\n"
      "v_sub_u32 v48, v39, v48\n"
      "v_lshlrev_b32 v48, 4, v48\n"
      "v_mul_u32_u24 v49, 1152, v47\n"
      "v_add_u32 v49, v49, v48\n"
      "v_mul_u32_u24 v47, 1168, v47\n"
      "v_add_u32 v60, v47, v48\n"
      "global_load_dwordx4 v[64:67], v49, s[78:79]\n"
      "v_add_u32 v39, 256, v38\n"
      "v_min_u32 v39, 575, v39\n"
      "v_mul_u32_u24 v47, 911, v39\n"
      "v_lshrrev_b32 v47, 16, v47\n"
      "v_mul_u32_u24 v48, 72, v47\n"
      "v_sub_u32 v48, v39, v48\n"
      "v_lshlrev_b32 v48, 4, v48\n"
      "v_mul_u32_u24 v49, 1152, v47\n"
      "v_add_u32 v49, v49, v48\n"
      "v_mul_u32_u24 v47, 1168, v47\n"
      "v_add_u32 v61, v47, v48\n"
      "global_load_dwordx4 v[68:71], v49, s[78:79]\n"
      "v_add_u32 v39, 512, v38\n"
      "v_min_u32 v39, 575, v39\n"
      "v_mul_u32_u24 v47, 911, v39\n"
      "v_lshrrev_b32 v47, 16, v47\n"
      "v_mul_u32_u24 v48, 72, v47\n"
      "v_sub_u32 v48, v39, v48\n"
      "v_lshlrev_b32 v48, 4, v48\n"
      "v_mul_u32_u24 v49, 1152, v47\n"
      "v_add_u32 v49, v49, v48\n"
      "v_mul_u32_u24 v47, 1168, v47\n"
      "v_add_u32 v63, v47, v48\n"
      "global_load_dwordx4 v[56:59], v49, s[78:79]\n"
      "v_mov_b32 v40, -1\n"
      "v_mov_b32 v41, -1\n"
      "v_mov_b32 v42, -1\n"
      "v_mov_b32 v43, -1\n"
      "s_add_u32 s44, s36, 0\n"
      "s_add_u32 s45, s44, s37\n"
      "s_add_u32 s46, s44, s38\n"
      "s_mov_b64 exec, -1\n"
      "buffer_store_dwordx4 v[40:43], v0, s[24:27], s45 offen sc1\n"
      "buffer_store_dwordx4 v[40:43], v0, s[24:27], s45 offen offset:1024 sc1\n"
      "s_mov_b32 exec_lo, 0xffff\n"
      "s_mov_b32 exec_hi, 0\n"
      "buffer_store_dwordx4 v[40:43], v0, s[24:27], s46 offen sc1\n"
      "s_add_u32 s44, s36, 589824\n"
      "s_add_u32 s45, s44, s37\n"
      "s_add_u32 s46, s44, s38\n"
      "s_mov_b64 exec, -1\n"
      "buffer_store_dwordx4 v[40:43], v0, s[24:27], s45 offen sc1\n"
      "buffer_store_dwordx4 v[40:43], v0, s[24:27], s45 offen offset:1024 sc1\n"
      "s_mov_b32 exec_lo, 0xffff\n"
      "s_mov_b32 exec_hi, 0\n"
      "buffer_store_dwordx4 v[40:43], v0, s[24:27], s46 offen sc1\n"
      "s_add_u32 s44, s36, 1179648\n"
      "s_add_u32 s45, s44, s37\n"
      "s_add_u32 s46, s44, s38\n"
      "s_mov_b64 exec, -1\n"
      "buffer_store_dwordx4 v[40:43], v0, s[24:27], s45 offen sc1\n"
      "buffer_store_dwordx4 v[40:43], v0, s[24:27], s45 offen offset:1024 sc1\n"
      "s_mov_b32 exec_lo, 0xffff\n"
      "s_mov_b32 exec_hi, 0\n"
      "buffer_store_dwordx4 v[40:43], v0, s[24:27], s46 offen sc1\n"
      "s_add_u32 s44, s36, 8192\n"
      "s_add_u32 s45, s44, s37\n"
      "s_add_u32 s46, s44, s38\n"
      "s_mov_b64 exec, -1\n"
      "buffer_store_dwordx4 v[40:43], v0, s[24:27], s45 offen sc1\n"
      "buffer_store_dwordx4 v[40:43], v0, s[24:27], s45 offen offset:1024 sc1\n"
      "s_mov_b32 exec_lo, 0xffff\n"
      "s_mov_b32 exec_hi, 0\n"
      "buffer_store_dwordx4 v[40:43], v0, s[24:27], s46 offen sc1\n"
      "s_add_u32 s44, s36, 598016\n"
      "s_add_u32 s45, s44, s37\n"
      "s_add_u32 s46, s44, s38\n"
      "s_mov_b64 exec, -1\n"
      "buffer_store_dwordx4 v[40:43], v0, s[24:27], s45 offen sc1\n"
      "buffer_store_dwordx4 v[40:43], v0, s[24:27], s45 offen offset:1024 sc1\n"
      "s_mov_b32 exec_lo, 0xffff\n"
      "s_mov_b32 exec_hi, 0\n"
      "buffer_store_dwordx4 v[40:43], v0, s[24:27], s46 offen sc1\n"
      "s_add_u32 s44, s36, 1187840\n"
      "s_add_u32 s45, s44, s37\n"
      "s_add_u32 s46, s44, s38\n"
      "s_mov_b64 exec, -1\n"
      "buffer_store_dwordx4 v[40:43], v0, s[24:27], s45 offen sc1\n"
      "buffer_store_dwordx4 v[40:43], v0, s[24:27], s45 offen offset:1024 sc1\n"
      "s_mov_b32 exec_lo, 0xffff\n"
      "s_mov_b32 exec_hi, 0\n"
      "buffer_store_dwordx4 v[40:43], v0, s[24:27], s46 offen sc1\n"
      "s_cmp_eq_u32 s43, -1\n"
      "s_cselect_b32 s47, s38, s66\n"
      "s_cmp_eq_u32 s43, -1\n"
      "s_cselect_b32 s44, 0, 0\n"
      "s_add_u32 s44, s44, 0\n"
      "s_add_u32 s44, s44, s36\n"
      "s_add_u32 s44, s44, s47\n"
      "buffer_store_dwordx4 v[40:43], v0, s[24:27], s44 offen sc1\n"
      "s_cmp_eq_u32 s43, -1\n"
      "s_cselect_b32 s44, 0, 32768\n"
      "s_add_u32 s44, s44, 0\n"
      "s_add_u32 s44, s44, s36\n"
      "s_add_u32 s44, s44, s47\n"
      "buffer_store_dwordx4 v[40:43], v0, s[24:27], s44 offen sc1\n"
      "s_cmp_eq_u32 s43, -1\n"
      "s_cselect_b32 s44, 0, 65536\n"
      "s_add_u32 s44, s44, 0\n"
      "s_add_u32 s44, s44, s36\n"
      "s_add_u32 s44, s44, s47\n"
      "buffer_store_dwordx4 v[40:43], v0, s[24:27], s44 offen sc1\n"
      "s_cmp_eq_u32 s43, -1\n"
      "s_cselect_b32 s44, 0, 0\n"
      "s_add_u32 s44, s44, 8192\n"
      "s_add_u32 s44, s44, s36\n"
      "s_add_u32 s44, s44, s47\n"
      "buffer_store_dwordx4 v[40:43], v0, s[24:27], s44 offen sc1\n"
      "s_cmp_eq_u32 s43, -1\n"
      "s_cselect_b32 s44, 0, 32768\n"
      "s_add_u32 s44, s44, 8192\n"
      "s_add_u32 s44, s44, s36\n"
      "s_add_u32 s44, s44, s47\n"
      "buffer_store_dwordx4 v[40:43], v0, s[24:27], s44 offen sc1\n"
      "s_cmp_eq_u32 s43, -1\n"
      "s_cselect_b32 s44, 0, 65536\n"
      "s_add_u32 s44, s44, 8192\n"
      "s_add_u32 s44, s44, s36\n"
      "s_add_u32 s44, s44, s47\n"
      "buffer_store_dwordx4 v[40:43], v0, s[24:27], s44 offen sc1\n"
      "s_mov_b64 exec, -1\n"
      "s_waitcnt vmcnt(51)\n"
      "s_barrier\n"
      "v_add_u32 v35, 0, v32\n"
      "ds_read_b64_tr_b16 a[0:1], v35 offset:0\n"
      "ds_read_b64_tr_b16 a[2:3], v35 offset:512\n"
      "ds_read_b64_tr_b16 a[4:5], v35 offset:4096\n"
      "ds_read_b64_tr_b16 a[6:7], v35 offset:4608\n"
      "v_add_u32 v35, 8192, v32\n"
      "ds_read_b64_tr_b16 a[8:9], v35 offset:0\n"
      "ds_read_b64_tr_b16 a[10:11], v35 offset:512\n"
      "ds_read_b64_tr_b16 a[12:13], v35 offset:4096\n"
      "ds_read_b64_tr_b16 a[14:15], v35 offset:4608\n"
      "v_add_u32 v35, 16384, v32\n"
      "ds_read_b64_tr_b16 a[16:17], v35 offset:0\n"
      "ds_read_b64_tr_b16 a[18:19], v35 offset:512\n"
      "ds_read_b64_tr_b16 a[20:21], v35 offset:4096\n"
      "ds_read_b64_tr_b16 a[22:23], v35 offset:4608\n"
      "s_waitcnt lgkmcnt(0)\n"
      "v_add_u32 v35, 24576, v32\n"
      "ds_read_b64_tr_b16 a[24:25], v35 offset:0\n"
      "ds_read_b64_tr_b16 a[26:27], v35 offset:512\n"
      "ds_read_b64_tr_b16 a[28:29], v35 offset:4096\n"
      "ds_read_b64_tr_b16 a[30:31], v35 offset:4608\n"
      "v_add_u32 v35, 32768, v32\n"
      "ds_read_b64_tr_b16 a[32:33], v35 offset:0\n"
      "ds_read_b64_tr_b16 a[34:35], v35 offset:512\n"
      "ds_read_b64_tr_b16 a[36:37], v35 offset:4096\n"
      "ds_read_b64_tr_b16 a[38:39], v35 offset:4608\n"
      "v_add_u32 v35, 40960, v32\n"
      "ds_read_b64_tr_b16 a[40:41], v35 offset:0\n"
      "ds_read_b64_tr_b16 a[42:43], v35 offset:512\n"
      "ds_read_b64_tr_b16 a[44:45], v35 offset:4096\n"
      "ds_read_b64_tr_b16 a[46:47], v35 offset:4608\n"
      "s_waitcnt lgkmcnt(0)\n"
      "s_waitcnt lgkmcnt(0)\n"
      "s_barrier\n"
      "s_mul_i32 s44, s34, 256\n"
      "s_add_u32 s28, s72, s44\n"
      "s_addc_u32 s29, s73, 0\n"
      "s_and_b32 s29, s29, 0xffff\n"
      "s_mov_b32 s30, 4710656\n"
      "s_mov_b32 s31, 0x20000\n"
      "s_mul_i32 s44, s57, 393216\n"
      "s_add_u32 s44, s44, 0\n"
      "s_mul_i32 s45, s57, 12288\n"
      "s_add_u32 m0, s45, 0\n"
      "s_nop 0\n"
      "buffer_load_dwordx4 v28, s[28:31], s44 offen lds\n"
      "s_add_u32 s44, s44, 32768\n"
      "s_add_u32 m0, m0, 1024\n"
      "s_nop 0\n"
      "buffer_load_dwordx4 v29, s[28:31], s44 offen lds\n"
      "s_add_u32 s44, s44, 32768\n"
      "s_add_u32 m0, m0, 1024\n"
      "s_nop 0\n"
      "buffer_load_dwordx4 v28, s[28:31], s44 offen lds\n"
      "s_add_u32 s44, s44, 32768\n"
      "s_add_u32 m0, m0, 1024\n"
      "s_nop 0\n"
      "buffer_load_dwordx4 v29, s[28:31], s44 offen lds\n"
      "s_add_u32 s44, s44, 32768\n"
      "s_add_u32 m0, m0, 1024\n"
      "s_nop 0\n"
      "buffer_load_dwordx4 v28, s[28:31], s44 offen lds\n"
      "s_add_u32 s44, s44, 32768\n"
      "s_add_u32 m0, m0, 1024\n"
      "s_nop 0\n"
      "buffer_load_dwordx4 v29, s[28:31], s44 offen lds\n"
      "s_add_u32 s44, s44, 32768\n"
      "s_add_u32 m0, m0, 1024\n"
      "s_nop 0\n"
      "buffer_load_dwordx4 v28, s[28:31], s44 offen lds\n"
      "s_add_u32 s44, s44, 32768\n"
      "s_add_u32 m0, m0, 1024\n"
      "s_nop 0\n"
      "buffer_load_dwordx4 v29, s[28:31], s44 offen lds\n"
      "s_add_u32 s44, s44, 32768\n"
      "s_add_u32 m0, m0, 1024\n"
      "s_nop 0\n"
      "buffer_load_dwordx4 v28, s[28:31], s44 offen lds\n"
      "s_add_u32 s44, s44, 32768\n"
      "s_add_u32 m0, m0, 1024\n"
      "s_nop 0\n"
      "buffer_load_dwordx4 v29, s[28:31], s44 offen lds\n"
      "s_add_u32 s44, s44, 32768\n"
      "s_add_u32 m0, m0, 1024\n"
      "s_nop 0\n"
      "buffer_load_dwordx4 v28, s[28:31], s44 offen lds\n"
      "s_add_u32 s44, s44, 32768\n"
      "s_add_u32 m0, m0, 1024\n"
      "s_nop 0\n"
      "buffer_load_dwordx4 v29, s[28:31], s44 offen lds\n"
      "s_waitcnt vmcnt(51)\n"
      "s_barrier\n"
      "v_add_u32 v35, 49152, v32\n"
      "ds_read_b64_tr_b16 a[48:49], v35 offset:0\n"
      "ds_read_b64_tr_b16 a[50:51], v35 offset:512\n"
      "ds_read_b64_tr_b16 a[52:53], v35 offset:4096\n"
      "ds_read_b64_tr_b16 a[54:55], v35 offset:4608\n"
      "v_add_u32 v35, 57344, v32\n"
      "ds_read_b64_tr_b16 a[56:57], v35 offset:0\n"
      "ds_read_b64_tr_b16 a[58:59], v35 offset:512\n"
      "ds_read_b64_tr_b16 a[60:61], v35 offset:4096\n"
      "ds_read_b64_tr_b16 a[62:63], v35 offset:4608\n"
      "v_add_u32 v35, 65536, v32\n"
      "ds_read_b64_tr_b16 a[64:65], v35 offset:0\n"
      "ds_read_b64_tr_b16 a[66:67], v35 offset:512\n"
      "ds_read_b64_tr_b16 a[68:69], v35 offset:4096\n"
      "ds_read_b64_tr_b16 a[70:71], v35 offset:4608\n"
      "s_waitcnt lgkmcnt(0)\n"
      "v_add_u32 v35, 73728, v32\n"
      "ds_read_b64_tr_b16 a[72:73], v35 offset:0\n"
      "ds_read_b64_tr_b16 a[74:75], v35 offset:512\n"
      "ds_read_b64_tr_b16 a[76:77], v35 offset:4096\n"
      "ds_read_b64_tr_b16 a[78:79], v35 offset:4608\n"
      "v_add_u32 v35, 81920, v32\n"
      "ds_read_b64_tr_b16 a[80:81], v35 offset:0\n"
      "ds_read_b64_tr_b16 a[82:83], v35 offset:512\n"
      "ds_read_b64_tr_b16 a[84:85], v35 offset:4096\n"
      "ds_read_b64_tr_b16 a[86:87], v35 offset:4608\n"
      "v_add_u32 v35, 90112, v32\n"
      "ds_read_b64_tr_b16 a[88:89], v35 offset:0\n"
      "ds_read_b64_tr_b16 a[90:91], v35 offset:512\n"
      "ds_read_b64_tr_b16 a[92:93], v35 offset:4096\n"
      "ds_read_b64_tr_b16 a[94:95], v35 offset:4608\n"
      "s_waitcnt lgkmcnt(0)\n"
      "s_waitcnt lgkmcnt(0)\n"
      "s_barrier\n"
      "s_mul_i32 s44, s34, 256\n"
      "s_add_u32 s28, s72, s44\n"
      "s_addc_u32 s29, s73, 0\n"
      "s_and_b32 s29, s29, 0xffff\n"
      "s_mov_b32 s30, 4710656\n"
      "s_mov_b32 s31, 0x20000\n"
      "s_mul_i32 s44, s57, 393216\n"
      "s_add_u32 s44, s44, 1572864\n"
      "s_mul_i32 s45, s57, 12288\n"
      "s_add_u32 m0, s45, 49152\n"
      "s_nop 0\n"
      "buffer_load_dwordx4 v28, s[28:31], s44 offen lds\n"
      "s_add_u32 s44, s44, 32768\n"
      "s_add_u32 m0, m0, 1024\n"
      "s_nop 0\n"
      "buffer_load_dwordx4 v29, s[28:31], s44 offen lds\n"
      "s_add_u32 s44, s44, 32768\n"
      "s_add_u32 m0, m0, 1024\n"
      "s_nop 0\n"
      "buffer_load_dwordx4 v28, s[28:31], s44 offen lds\n"
      "s_add_u32 s44, s44, 32768\n"
      "s_add_u32 m0, m0, 1024\n"
      "s_nop 0\n"
      "buffer_load_dwordx4 v29, s[28:31], s44 offen lds\n"
      "s_add_u32 s44, s44, 32768\n"
      "s_add_u32 m0, m0, 1024\n"
      "s_nop 0\n"
      "buffer_load_dwordx4 v28, s[28:31], s44 offen lds\n"
      "s_add_u32 s44, s44, 32768\n"
      "s_add_u32 m0, m0, 1024\n"
      "s_nop 0\n"
      "buffer_load_dwordx4 v29, s[28:31], s44 offen lds\n"
      "s_add_u32 s44, s44, 32768\n"
      "s_add_u32 m0, m0, 1024\n"
      "s_nop 0\n"
      "buffer_load_dwordx4 v28, s[28:31], s44 offen lds\n"
      "s_add_u32 s44, s44, 32768\n"
      "s_add_u32 m0, m0, 1024\n"
      "s_nop 0\n"
      "buffer_load_dwordx4 v29, s[28:31], s44 offen lds\n"
      "s_add_u32 s44, s44, 32768\n"
      "s_add_u32 m0, m0, 1024\n"
      "s_nop 0\n"
      "buffer_load_dwordx4 v28, s[28:31], s44 offen lds\n"
      "s_add_u32 s44, s44, 32768\n"
      "s_add_u32 m0, m0, 1024\n"
      "s_nop 0\n"
      "buffer_load_dwordx4 v29, s[28:31], s44 offen lds\n"
      "s_add_u32 s44, s44, 32768\n"
      "s_add_u32 m0, m0, 1024\n"
      "s_nop 0\n"
      "buffer_load_dwordx4 v28, s[28:31], s44 offen lds\n"
      "s_add_u32 s44, s44, 32768\n"
      "s_add_u32 m0, m0, 1024\n"
      "s_nop 0\n"
      "buffer_load_dwordx4 v29, s[28:31], s44 offen lds\n"
      "s_waitcnt vmcnt(51)\n"
      "s_barrier\n"
      "v_add_u32 v35, 98304, v32\n"
      "ds_read_b64_tr_b16 a[96:97], v35 offset:0\n"
      "ds_read_b64_tr_b16 a[98:99], v35 offset:512\n"
      "ds_read_b64_tr_b16 a[100:101], v35 offset:4096\n"
      "ds_read_b64_tr_b16 a[102:103], v35 offset:4608\n"
      "v_add_u32 v35, 106496, v32\n"
      "ds_read_b64_tr_b16 a[104:105], v35 offset:0\n"
      "ds_read_b64_tr_b16 a[106:107], v35 offset:512\n"
      "ds_read_b64_tr_b16 a[108:109], v35 offset:4096\n"
      "ds_read_b64_tr_b16 a[110:111], v35 offset:4608\n"
      "v_add_u32 v35, 114688, v32\n"
      "ds_read_b64_tr_b16 a[112:113], v35 offset:0\n"
      "ds_read_b64_tr_b16 a[114:115], v35 offset:512\n"
      "ds_read_b64_tr_b16 a[116:117], v35 offset:4096\n"
      "ds_read_b64_tr_b16 a[118:119], v35 offset:4608\n"
      "s_waitcnt lgkmcnt(0)\n"
      "v_add_u32 v35, 122880, v32\n"
      "ds_read_b64_tr_b16 a[120:121], v35 offset:0\n"
      "ds_read_b64_tr_b16 a[122:123], v35 offset:512\n"
      "ds_read_b64_tr_b16 a[124:125], v35 offset:4096\n"
      "ds_read_b64_tr_b16 a[126:127], v35 offset:4608\n"
      "v_add_u32 v35, 131072, v32\n"
      "ds_read_b64_tr_b16 a[128:129], v35 offset:0\n"
      "ds_read_b64_tr_b16 a[130:131], v35 offset:512\n"
      "ds_read_b64_tr_b16 a[132:133], v35 offset:4096\n"
      "ds_read_b64_tr_b16 a[134:135], v35 offset:4608\n"
      "v_add_u32 v35, 139264, v32\n"
      "ds_read_b64_tr_b16 a[136:137], v35 offset:0\n"
      "ds_read_b64_tr_b16 a[138:139], v35 offset:512\n"
      "ds_read_b64_tr_b16 a[140:141], v35 offset:4096\n"
      "ds_read_b64_tr_b16 a[142:143], v35 offset:4608\n"
      "s_waitcnt lgkmcnt(0)\n"
      "s_waitcnt lgkmcnt(0)\n"
      "s_barrier\n"
      "s_mul_i32 s44, s34, 256\n"
      "s_add_u32 s28, s72, s44\n"
      "s_addc_u32 s29, s73, 0\n"
      "s_and_b32 s29, s29, 0xffff\n"
      "s_mov_b32 s30, 4710656\n"
      "s_mov_b32 s31, 0x20000\n"
      "s_mul_i32 s44, s57, 393216\n"
      "s_add_u32 s44, s44, 3145728\n"
      "s_mul_i32 s45, s57, 12288\n"
      "s_add_u32 m0, s45, 98304\n"
      "s_nop 0\n"
      "buffer_load_dwordx4 v28, s[28:31], s44 offen lds\n"
      "s_add_u32 s44, s44, 32768\n"
      "s_add_u32 m0, m0, 1024\n"
      "s_nop 0\n"
      "buffer_load_dwordx4 v29, s[28:31], s44 offen lds\n"
      "s_add_u32 s44, s44, 32768\n"
      "s_add_u32 m0, m0, 1024\n"
      "s_nop 0\n"
      "buffer_load_dwordx4 v28, s[28:31], s44 offen lds\n"
      "s_add_u32 s44, s44, 32768\n"
      "s_add_u32 m0, m0, 1024\n"
      "s_nop 0\n"
      "buffer_load_dwordx4 v29, s[28:31], s44 offen lds\n"
      "s_add_u32 s44, s44, 32768\n"
      "s_add_u32 m0, m0, 1024\n"
      "s_nop 0\n"
      "buffer_load_dwordx4 v28, s[28:31], s44 offen lds\n"
      "s_add_u32 s44, s44, 32768\n"
      "s_add_u32 m0, m0, 1024\n"
      "s_nop 0\n"
      "buffer_load_dwordx4 v29, s[28:31], s44 offen lds\n"
      "s_add_u32 s44, s44, 32768\n"
      "s_add_u32 m0, m0, 1024\n"
      "s_nop 0\n"
      "buffer_load_dwordx4 v28, s[28:31], s44 offen lds\n"
      "s_add_u32 s44, s44, 32768\n"
      "s_add_u32 m0, m0, 1024\n"
      "s_nop 0\n"
      "buffer_load_dwordx4 v29, s[28:31], s44 offen lds\n"
      "s_add_u32 s44, s44, 32768\n"
      "s_add_u32 m0, m0, 1024\n"
      "s_nop 0\n"
      "buffer_load_dwordx4 v28, s[28:31], s44 offen lds\n"
      "s_add_u32 s44, s44, 32768\n"
      "s_add_u32 m0, m0, 1024\n"
      "s_nop 0\n"
      "buffer_load_dwordx4 v29, s[28:31], s44 offen lds\n"
      "s_add_u32 s44, s44, 32768\n"
      "s_add_u32 m0, m0, 1024\n"
      "s_nop 0\n"
      "buffer_load_dwordx4 v28, s[28:31], s44 offen lds\n"
      "s_add_u32 s44, s44, 32768\n"
      "s_add_u32 m0, m0, 1024\n"
      "s_nop 0\n"
      "buffer_load_dwordx4 v29, s[28:31], s44 offen lds\n"
      "s_waitcnt vmcnt(24)\n"
      "s_barrier\n"
      "s_cmp_lg_u32 s57, 0\n"
      "s_cbranch_scc1 L_noarr_%=\n"
      "s_lshl_b32 s46, s58, 8\n"
      "s_add_u32 s46, s46, 0\n"
      "v_mov_b32 v24, 1\n"
      "s_mov_b32 exec_lo, 1\n"
      "s_mov_b32 exec_hi, 0\n"
      "buffer_atomic_add v24, off, s[24:27], s46 sc1\n"
      "s_mov_b64 exec, -1\n"
      "L_noarr_%=:\n"
      "v_add_u32 v35, 0, v32\n"
      "ds_read_b64_tr_b16 a[144:145], v35 offset:0\n"
      "ds_read_b64_tr_b16 a[146:147], v35 offset:512\n"
      "ds_read_b64_tr_b16 a[148:149], v35 offset:4096\n"
      "ds_read_b64_tr_b16 a[150:151], v35 offset:4608\n"
      "v_add_u32 v35, 8192, v32\n"
      "ds_read_b64_tr_b16 a[152:153], v35 offset:0\n"
      "ds_read_b64_tr_b16 a[154:155], v35 offset:512\n"
      "ds_read_b64_tr_b16 a[156:157], v35 offset:4096\n"
      "ds_read_b64_tr_b16 a[158:159], v35 offset:4608\n"
      "v_add_u32 v35, 16384, v32\n"
      "ds_read_b64_tr_b16 a[160:161], v35 offset:0\n"
      "ds_read_b64_tr_b16 a[162:163], v35 offset:512\n"
      "ds_read_b64_tr_b16 a[164:165], v35 offset:4096\n"
      "ds_read_b64_tr_b16 a[166:167], v35 offset:4608\n"
      "s_waitcnt lgkmcnt(0)\n"
      "v_add_u32 v35, 24576, v32\n"
      "ds_read_b64_tr_b16 a[168:169], v35 offset:0\n"
      "ds_read_b64_tr_b16 a[170:171], v35 offset:512\n"
      "ds_read_b64_tr_b16 a[172:173], v35 offset:4096\n"
      "ds_read_b64_tr_b16 a[174:175], v35 offset:4608\n"
      "v_add_u32 v35, 32768, v32\n"
      "ds_read_b64_tr_b16 a[176:177], v35 offset:0\n"
      "ds_read_b64_tr_b16 a[178:179], v35 offset:512\n"
      "ds_read_b64_tr_b16 a[180:181], v35 offset:4096\n"
      "ds_read_b64_tr_b16 a[182:183], v35 offset:4608\n"
      "v_add_u32 v35, 40960, v32\n"
      "ds_read_b64_tr_b16 a[184:185], v35 offset:0\n"
      "ds_read_b64_tr_b16 a[186:187], v35 offset:512\n"
      "ds_read_b64_tr_b16 a[188:189], v35 offset:4096\n"
      "ds_read_b64_tr_b16 a[190:191], v35 offset:4608\n"
      "s_waitcnt lgkmcnt(0)\n"
      "s_waitcnt lgkmcnt(0)\n"
      "s_barrier\n"
      "s_mov_b32 s30, 4710656\n"
      "s_mul_i32 s44, s34, 256\n"
      "s_add_u32 s28, s74, s44\n"
      "s_addc_u32 s29, s75, 0\n"
      "s_and_b32 s29, s29, 0xffff\n"
      "s_mul_i32 s45, s57, 4\n"
      "s_add_u32 s45, s45, 120\n"
      "s_lshl_b32 s44, s45, 15\n"
      "s_lshl_b32 s45, s45, 10\n"
      "s_add_u32 m0, s45, 24576\n"
      "s_nop 0\n"
      "buffer_load_dwordx4 v28, s[28:31], s44 offen lds\n"
      "s_add_u32 s44, s44, 32768\n"
      "s_add_u32 m0, m0, 1024\n"
      "s_nop 0\n"
      "buffer_load_dwordx4 v29, s[28:31], s44 offen lds\n"
      "s_add_u32 s44, s44, 32768\n"
      "s_add_u32 m0, m0, 1024\n"
      "s_nop 0\n"
      "buffer_load_dwordx4 v28, s[28:31], s44 offen lds\n"
      "s_add_u32 s44, s44, 32768\n"
      "s_add_u32 m0, m0, 1024\n"
      "s_nop 0\n"
      "buffer_load_dwordx4 v29, s[28:31], s44 offen lds\n"
      "s_waitcnt vmcnt(16)\n"
      "s_barrier\n"
      "v_add_u32 v35, 49152, v32\n"
      "ds_read_b64_tr_b16 a[192:193], v35 offset:0\n"
      "ds_read_b64_tr_b16 a[194:195], v35 offset:512\n"
      "ds_read_b64_tr_b16 a[196:197], v35 offset:4096\n"
      "ds_read_b64_tr_b16 a[198:199], v35 offset:4608\n"
      "v_add_u32 v35, 57344, v32\n"
      "ds_read_b64_tr_b16 a[200:201], v35 offset:0\n"
      "ds_read_b64_tr_b16 a[202:203], v35 offset:512\n"
      "ds_read_b64_tr_b16 a[204:205], v35 offset:4096\n"
      "ds_read_b64_tr_b16 a[206:207], v35 offset:4608\n"
      "v_add_u32 v35, 65536, v32\n"
      "ds_read_b64_tr_b16 a[208:209], v35 offset:0\n"
      "ds_read_b64_tr_b16 a[210:211], v35 offset:512\n"
      "ds_read_b64_tr_b16 a[212:213], v35 offset:4096\n"
      "ds_read_b64_tr_b16 a[214:215], v35 offset:4608\n"
      "s_waitcnt lgkmcnt(0)\n"
      "v_add_u32 v35, 73728, v32\n"
      "ds_read_b64_tr_b16 a[216:217], v35 offset:0\n"
      "ds_read_b64_tr_b16 a[218:219], v35 offset:512\n"
      "ds_read_b64_tr_b16 a[220:221], v35 offset:4096\n"
      "ds_read_b64_tr_b16 a[222:223], v35 offset:4608\n"
      "v_add_u32 v35, 81920, v32\n"
      "ds_read_b64_tr_b16 a[224:225], v35 offset:0\n"
      "ds_read_b64_tr_b16 a[226:227], v35 offset:512\n"
      "ds_read_b64_tr_b16 a[228:229], v35 offset:4096\n"
      "ds_read_b64_tr_b16 a[230:231], v35 offset:4608\n"
      "v_add_u32 v35, 90112, v32\n"
      "ds_read_b64_tr_b16 a[232:233], v35 offset:0\n"
      "ds_read_b64_tr_b16 a[234:235], v35 offset:512\n"
      "ds_read_b64_tr_b16 a[236:237], v35 offset:4096\n"
      "ds_read_b64_tr_b16 a[238:239], v35 offset:4608\n"
      "s_waitcnt lgkmcnt(0)\n"
      "s_waitcnt lgkmcnt(0)\n"
      "s_barrier\n"
      "s_mul_i32 s44, s34, 147456\n"
      "s_add_u32 s28, s76, s44\n"
      "s_addc_u32 s29, s77, 0\n"
      "s_and_b32 s29, s29, 0xffff\n"
      "s_mov_b32 s30, 147456\n"
      "s_mov_b32 s31, 0x20000\n"
      "s_mul_i32 s44, s57, 18432\n"
      "s_add_u32 s44, s44, 0\n"
      "s_mov_b32 m0, s44\n"
      "s_nop 0\n"
      "buffer_load_dwordx4 v0, s[28:31], s44 offen lds\n"
      "s_add_u32 s44, s44, 1024\n"
      "s_add_u32 m0, m0, 1024\n"
      "s_nop 0\n"
      "buffer_load_dwordx4 v0, s[28:31], s44 offen lds\n"
      "s_add_u32 s44, s44, 1024\n"
      "s_add_u32 m0, m0, 1024\n"
      "s_nop 0\n"
      "buffer_load_dwordx4 v0, s[28:31], s44 offen lds\n"
      "s_add_u32 s44, s44, 1024\n"
      "s_add_u32 m0, m0, 1024\n"
      "s_nop 0\n"
      "buffer_load_dwordx4 v0, s[28:31], s44 offen lds\n"
      "s_add_u32 s44, s44, 1024\n"
      "s_add_u32 m0, m0, 1024\n"
      "s_nop 0\n"
      "buffer_load_dwordx4 v0, s[28:31], s44 offen lds\n"
      "s_add_u32 s44, s44, 1024\n"
      "s_add_u32 m0, m0, 1024\n"
      "s_nop 0\n"
      "buffer_load_dwordx4 v0, s[28:31], s44 offen lds\n"
      "s_add_u32 s44, s44, 1024\n"
      "s_add_u32 m0, m0, 1024\n"
      "s_nop 0\n"
      "buffer_load_dwordx4 v0, s[28:31], s44 offen lds\n"
      "s_add_u32 s44, s44, 1024\n"
      "s_add_u32 m0, m0, 1024\n"
      "s_nop 0\n"
      "buffer_load_dwordx4 v0, s[28:31], s44 offen lds\n"
      "s_add_u32 s44, s44, 1024\n"
      "s_add_u32 m0, m0, 1024\n"
      "s_nop 0\n"
      "buffer_load_dwordx4 v0, s[28:31], s44 offen lds\n"
      "s_add_u32 s44, s44, 1024\n"
      "s_add_u32 m0, m0, 1024\n"
      "s_nop 0\n"
      "buffer_load_dwordx4 v0, s[28:31], s44 offen lds\n"
      "s_add_u32 s44, s44, 1024\n"
      "s_add_u32 m0, m0, 1024\n"
      "s_nop 0\n"
      "buffer_load_dwordx4 v0, s[28:31], s44 offen lds\n"
      "s_add_u32 s44, s44, 1024\n"
      "s_add_u32 m0, m0, 1024\n"
      "s_nop 0\n"
      "buffer_load_dwordx4 v0, s[28:31], s44 offen lds\n"
      "s_add_u32 s44, s44, 1024\n"
      "s_add_u32 m0, m0, 1024\n"
      "s_nop 0\n"
      "buffer_load_dwordx4 v0, s[28:31], s44 offen lds\n"
      "s_add_u32 s44, s44, 1024\n"
      "s_add_u32 m0, m0, 1024\n"
      "s_nop 0\n"
      "buffer_load_dwordx4 v0, s[28:31], s44 offen lds\n"
      "s_add_u32 s44, s44, 1024\n"
      "s_add_u32 m0, m0, 1024\n"
      "s_nop 0\n"
      "buffer_load_dwordx4 v0, s[28:31], s44 offen lds\n"
      "s_add_u32 s44, s44, 1024\n"
      "s_add_u32 m0, m0, 1024\n"
      "s_nop 0\n"
      "buffer_load_dwordx4 v0, s[28:31], s44 offen lds\n"
      "s_add_u32 s44, s44, 1024\n"
      "s_add_u32 m0, m0, 1024\n"
      "s_nop 0\n"
      "buffer_load_dwordx4 v0, s[28:31], s44 offen lds\n"
      "s_add_u32 s44, s44, 1024\n"
      "s_add_u32 m0, m0, 1024\n"
      "s_nop 0\n"
      "buffer_load_dwordx4 v0, s[28:31], s44 offen lds\n"
      "s_waitcnt vmcnt(22)\n"
      "s_barrier\n"
      "v_add_u32 v35, 98304, v32\n"
      "ds_read_b64_tr_b16 a[240:241], v35 offset:0\n"
      "ds_read_b64_tr_b16 a[242:243], v35 offset:512\n"
      "ds_read_b64_tr_b16 a[244:245], v35 offset:4096\n"
      "ds_read_b64_tr_b16 a[246:247], v35 offset:4608\n"
      "v_add_u32 v35, 106496, v32\n"
      "ds_read_b64_tr_b16 a[248:249], v35 offset:0\n"
      "ds_read_b64_tr_b16 a[250:251], v35 offset:512\n"
      "ds_read_b64_tr_b16 a[252:253], v35 offset:4096\n"
      "ds_read_b64_tr_b16 a[254:255], v35 offset:4608\n"
      "v_add_u32 v35, 114688, v32\n"
      "ds_read_b64_tr_b16 v[72:73], v35 offset:0\n"
      "ds_read_b64_tr_b16 v[74:75], v35 offset:512\n"
      "ds_read_b64_tr_b16 v[76:77], v35 offset:4096\n"
      "ds_read_b64_tr_b16 v[78:79], v35 offset:4608\n"
      "s_waitcnt lgkmcnt(0)\n"
      "v_add_u32 v35, 122880, v32\n"
      "ds_read_b64_tr_b16 v[80:81], v35 offset:0\n"
      "ds_read_b64_tr_b16 v[82:83], v35 offset:512\n"
      "ds_read_b64_tr_b16 v[84:85], v35 offset:4096\n"
      "ds_read_b64_tr_b16 v[86:87], v35 offset:4608\n"
      "v_add_u32 v35, 131072, v32\n"
      "ds_read_b64_tr_b16 v[88:89], v35 offset:0\n"
      "ds_read_b64_tr_b16 v[90:91], v35 offset:512\n"
      "ds_read_b64_tr_b16 v[92:93], v35 offset:4096\n"
      "ds_read_b64_tr_b16 v[94:95], v35 offset:4608\n"
      "v_add_u32 v35, 139264, v32\n"
      "ds_read_b64_tr_b16 v[96:97], v35 offset:0\n"
      "ds_read_b64_tr_b16 v[98:99], v35 offset:512\n"
      "ds_read_b64_tr_b16 v[100:101], v35 offset:4096\n"
      "ds_read_b64_tr_b16 v[102:103], v35 offset:4608\n"
      "s_waitcnt lgkmcnt(0)\n"
      "s_waitcnt lgkmcnt(0)\n"
      "s_barrier\n"
      "s_mul_i32 s44, s34, 147456\n"
      "s_add_u32 s28, s76, s44\n"
      "s_addc_u32 s29, s77, 0\n"
      "s_and_b32 s29, s29, 0xffff\n"
      "s_mov_b32 s30, 147456\n"
      "s_mov_b32 s31, 0x20000\n"
      "s_mul_i32 s44, s57, 18432\n"
      "s_add_u32 s44, s44, 73728\n"
      "s_mov_b32 m0, s44\n"
      "s_nop 0\n"
      "buffer_load_dwordx4 v0, s[28:31], s44 offen lds\n"
      "s_add_u32 s44, s44, 1024\n"
      "s_add_u32 m0, m0, 1024\n"
      "s_nop 0\n"
      "buffer_load_dwordx4 v0, s[28:31], s44 offen lds\n"
      "s_add_u32 s44, s44, 1024\n"
      "s_add_u32 m0, m0, 1024\n"
      "s_nop 0\n"
      "buffer_load_dwordx4 v0, s[28:31], s44 offen lds\n"
      "s_add_u32 s44, s44, 1024\n"
      "s_add_u32 m0, m0, 1024\n"
      "s_nop 0\n"
      "buffer_load_dwordx4 v0, s[28:31], s44 offen lds\n"
      "s_add_u32 s44, s44, 1024\n"
      "s_add_u32 m0, m0, 1024\n"
      "s_nop 0\n"
      "buffer_load_dwordx4 v0, s[28:31], s44 offen lds\n"
      "s_add_u32 s44, s44, 1024\n"
      "s_add_u32 m0, m0, 1024\n"
      "s_nop 0\n"
      "buffer_load_dwordx4 v0, s[28:31], s44 offen lds\n"
      "s_add_u32 s44, s44, 1024\n"
      "s_add_u32 m0, m0, 1024\n"
      "s_nop 0\n"
      "buffer_load_dwordx4 v0, s[28:31], s44 offen lds\n"
      "s_add_u32 s44, s44, 1024\n"
      "s_add_u32 m0, m0, 1024\n"
      "s_nop 0\n"
      "buffer_load_dwordx4 v0, s[28:31], s44 offen lds\n"
      "s_add_u32 s44, s44, 1024\n"
      "s_add_u32 m0, m0, 1024\n"
      "s_nop 0\n"
      "buffer_load_dwordx4 v0, s[28:31], s44 offen lds\n"
      "s_add_u32 s44, s44, 1024\n"
      "s_add_u32 m0, m0, 1024\n"
      "s_nop 0\n"
      "buffer_load_dwordx4 v0, s[28:31], s44 offen lds\n"
      "s_add_u32 s44, s44, 1024\n"
      "s_add_u32 m0, m0, 1024\n"
      "s_nop 0\n"
      "buffer_load_dwordx4 v0, s[28:31], s44 offen lds\n"
      "s_add_u32 s44, s44, 1024\n"
      "s_add_u32 m0, m0, 1024\n"
      "s_nop 0\n"
      "buffer_load_dwordx4 v0, s[28:31], s44 offen lds\n"
      "s_add_u32 s44, s44, 1024\n"
      "s_add_u32 m0, m0, 1024\n"
      "s_nop 0\n"
      "buffer_load_dwordx4 v0, s[28:31], s44 offen lds\n"
      "s_add_u32 s44, s44, 1024\n"
      "s_add_u32 m0, m0, 1024\n"
      "s_nop 0\n"
      "buffer_load_dwordx4 v0, s[28:31], s44 offen lds\n"
      "s_add_u32 s44, s44, 1024\n"
      "s_add_u32 m0, m0, 1024\n"
      "s_nop 0\n"
      "buffer_load_dwordx4 v0, s[28:31], s44 offen lds\n"
      "s_add_u32 s44, s44, 1024\n"
      "s_add_u32 m0, m0, 1024\n"
      "s_nop 0\n"
      "buffer_load_dwordx4 v0, s[28:31], s44 offen lds\n"
      "s_add_u32 s44, s44, 1024\n"
      "s_add_u32 m0, m0, 1024\n"
      "s_nop 0\n"
      "buffer_load_dwordx4 v0, s[28:31], s44 offen lds\n"
      "s_add_u32 s44, s44, 1024\n"
      "s_add_u32 m0, m0, 1024\n"
      "s_nop 0\n"
      "buffer_load_dwordx4 v0, s[28:31], s44 offen lds\n"
      "s_waitcnt vmcnt(18)\n"
      "s_barrier\n"
      "v_add_u32 v35, 0, v34\n"
      "v_add_u32 v37, 0, v36\n"
      "ds_read_b64_tr_b16 v[104:105], v35 offset:0\n"
      "ds_read_b64_tr_b16 v[106:107], v35 offset:2304\n"
      "ds_read_b64_tr_b16 v[108:109], v35 offset:9216\n"
      "ds_read_b64_tr_b16 v[110:111], v35 offset:11520\n"
      "ds_read_b64_tr_b16 v[112:113], v35 offset:36864\n"
      "ds_read_b64_tr_b16 v[114:115], v35 offset:39168\n"
      "ds_read_b64_tr_b16 v[116:117], v35 offset:46080\n"
      "ds_read_b64_tr_b16 v[118:119], v35 offset:48384\n"
      "s_waitcnt lgkmcnt(0)\n"
      "ds_read_b64_tr_b16 v[136:137], v35 offset:32\n"
      "ds_read_b64_tr_b16 v[138:139], v35 offset:2336\n"
      "ds_read_b64_tr_b16 v[140:141], v35 offset:9248\n"
      "ds_read_b64_tr_b16 v[142:143], v35 offset:11552\n"
      "ds_read_b64_tr_b16 v[144:145], v35 offset:36896\n"
      "ds_read_b64_tr_b16 v[146:147], v35 offset:39200\n"
      "ds_read_b64_tr_b16 v[148:149], v35 offset:46112\n"
      "ds_read_b64_tr_b16 v[150:151], v35 offset:48416\n"
      "s_waitcnt lgkmcnt(0)\n"
      "ds_read_b64_tr_b16 v[168:169], v35 offset:128\n"
      "ds_read_b64_tr_b16 v[170:171], v35 offset:2432\n"
      "ds_read_b64_tr_b16 v[172:173], v35 offset:9344\n"
      "ds_read_b64_tr_b16 v[174:175], v35 offset:11648\n"
      "ds_read_b64_tr_b16 v[176:177], v35 offset:36992\n"
      "ds_read_b64_tr_b16 v[178:179], v35 offset:39296\n"
      "ds_read_b64_tr_b16 v[180:181], v35 offset:46208\n"
      "ds_read_b64_tr_b16 v[182:183], v35 offset:48512\n"
      "s_waitcnt lgkmcnt(0)\n"
      "ds_read_b64_tr_b16 v[216:217], v35 offset:160\n"
      "ds_read_b64_tr_b16 v[218:219], v35 offset:2464\n"
      "ds_read_b64_tr_b16 v[220:221], v35 offset:9376\n"
      "ds_read_b64_tr_b16 v[222:223], v35 offset:11680\n"
      "ds_read_b64_tr_b16 v[224:225], v35 offset:37024\n"
      "ds_read_b64_tr_b16 v[226:227], v35 offset:39328\n"
      "ds_read_b64_tr_b16 v[228:229], v35 offset:46240\n"
      "ds_read_b64_tr_b16 v[230:231], v35 offset:48544\n"
      "s_waitcnt lgkmcnt(0)\n"
      "ds_read_b64_tr_b16 v[200:201], v37 offset:0\n"
      "ds_read_b64_tr_b16 v[202:203], v37 offset:2304\n"
      "ds_read_b64_tr_b16 v[204:205], v37 offset:9216\n"
      "ds_read_b64_tr_b16 v[206:207], v37 offset:11520\n"
      "s_waitcnt lgkmcnt(0)\n"
      "s_barrier\n"
      "s_mov_b32 s30, 4710656\n"
      "s_mul_i32 s44, s34, 256\n"
      "s_add_u32 s28, s74, s44\n"
      "s_addc_u32 s29, s75, 0\n"
      "s_and_b32 s29, s29, 0xffff\n"
      "s_mul_i32 s45, s57, 12\n"
      "s_add_u32 s45, s45, 0\n"
      "s_lshl_b32 s44, s45, 15\n"
      "s_lshl_b32 s45, s45, 10\n"
      "s_add_u32 m0, s45, 24576\n"
      "s_nop 0\n"
      "buffer_load_dwordx4 v28, s[28:31], s44 offen lds\n"
      "s_add_u32 s44, s44, 32768\n"
      "s_add_u32 m0, m0, 1024\n"
      "s_nop 0\n"
      "buffer_load_dwordx4 v29, s[28:31], s44 offen lds\n"
      "s_add_u32 s44, s44, 32768\n"
      "s_add_u32 m0, m0, 1024\n"
      "s_nop 0\n"
      "buffer_load_dwordx4 v28, s[28:31], s44 offen lds\n"
      "s_add_u32 s44, s44, 32768\n"
      "s_add_u32 m0, m0, 1024\n"
      "s_nop 0\n"
      "buffer_load_dwordx4 v29, s[28:31], s44 offen lds\n"
      "s_add_u32 s44, s44, 32768\n"
      "s_add_u32 m0, m0, 1024\n"
      "s_nop 0\n"
      "buffer_load_dwordx4 v28, s[28:31], s44 offen lds\n"
      "s_add_u32 s44, s44, 32768\n"
      "s_add_u32 m0, m0, 1024\n"
      "s_nop 0\n"
      "buffer_load_dwordx4 v29, s[28:31], s44 offen lds\n"
      "s_add_u32 s44, s44, 32768\n"
      "s_add_u32 m0, m0, 1024\n"
      "s_nop 0\n"
      "buffer_load_dwordx4 v28, s[28:31], s44 offen lds\n"
      "s_add_u32 s44, s44, 32768\n"
      "s_add_u32 m0, m0, 1024\n"
      "s_nop 0\n"
      "buffer_load_dwordx4 v29, s[28:31], s44 offen lds\n"
      "s_add_u32 s44, s44, 32768\n"
      "s_add_u32 m0, m0, 1024\n"
      "s_nop 0\n"
      "buffer_load_dwordx4 v28, s[28:31], s44 offen lds\n"
      "s_add_u32 s44, s44, 32768\n"
      "s_add_u32 m0, m0, 1024\n"
      "s_nop 0\n"
      "buffer_load_dwordx4 v29, s[28:31], s44 offen lds\n"
      "s_add_u32 s44, s44, 32768\n"
      "s_add_u32 m0, m0, 1024\n"
      "s_nop 0\n"
      "buffer_load_dwordx4 v28, s[28:31], s44 offen lds\n"
      "s_add_u32 s44, s44, 32768\n"
      "s_add_u32 m0, m0, 1024\n"
      "s_nop 0\n"
      "buffer_load_dwordx4 v29, s[28:31], s44 offen lds\n"
      "s_mov_b32 s30, 4710656\n"
      "s_mul_i32 s44, s34, 256\n"
      "s_add_u32 s28, s74, s44\n"
      "s_addc_u32 s29, s75, 0\n"
      "s_and_b32 s29, s29, 0xffff\n"
      "s_mul_i32 s45, s57, 2\n"
      "s_add_u32 s45, s45, 136\n"
      "s_lshl_b32 s44, s45, 15\n"
      "s_lshl_b32 s45, s45, 10\n"
      "s_add_u32 m0, s45, -125696\n"
      "s_nop 0\n"
      "buffer_load_dwordx4 v28, s[28:31], s44 offen lds\n"
      "s_add_u32 s44, s44, 32768\n"
      "s_add_u32 m0, m0, 1024\n"
      "s_nop 0\n"
      "buffer_load_dwordx4 v29, s[28:31], s44 offen lds\n"
      "s_waitcnt vmcnt(14)\n"
      "s_barrier\n"
      "v_add_u32 v35, 73728, v34\n"
      "v_add_u32 v37, 73728, v36\n"
      "ds_read_b64_tr_b16 v[120:121], v35 offset:0\n"
      "ds_read_b64_tr_b16 v[122:123], v35 offset:2304\n"
      "ds_read_b64_tr_b16 v[124:125], v35 offset:9216\n"
      "ds_read_b64_tr_b16 v[126:127], v35 offset:11520\n"
      "ds_read_b64_tr_b16 v[128:129], v35 offset:36864\n"
      "ds_read_b64_tr_b16 v[130:131], v35 offset:39168\n"
      "ds_read_b64_tr_b16 v[132:133], v35 offset:46080\n"
      "ds_read_b64_tr_b16 v[134:135], v35 offset:48384\n"
      "s_waitcnt lgkmcnt(0)\n"
      "ds_read_b64_tr_b16 v[152:153], v35 offset:32\n"
      "ds_read_b64_tr_b16 v[154:155], v35 offset:2336\n"
      "ds_read_b64_tr_b16 v[156:157], v35 offset:9248\n"
      "ds_read_b64_tr_b16 v[158:159], v35 offset:11552\n"
      "ds_read_b64_tr_b16 v[160:161], v35 offset:36896\n"
      "ds_read_b64_tr_b16 v[162:163], v35 offset:39200\n"
      "ds_read_b64_tr_b16 v[164:165], v35 offset:46112\n"
      "ds_read_b64_tr_b16 v[166:167], v35 offset:48416\n"
      "s_waitcnt lgkmcnt(0)\n"
      "ds_read_b64_tr_b16 v[184:185], v35 offset:128\n"
      "ds_read_b64_tr_b16 v[186:187], v35 offset:2432\n"
      "ds_read_b64_tr_b16 v[188:189], v35 offset:9344\n"
      "ds_read_b64_tr_b16 v[190:191], v35 offset:11648\n"
      "ds_read_b64_tr_b16 v[192:193], v35 offset:36992\n"
      "ds_read_b64_tr_b16 v[194:195], v35 offset:39296\n"
      "ds_read_b64_tr_b16 v[196:197], v35 offset:46208\n"
      "ds_read_b64_tr_b16 v[198:199], v35 offset:48512\n"
      "s_waitcnt lgkmcnt(0)\n"
      "ds_read_b64_tr_b16 v[232:233], v35 offset:160\n"
      "ds_read_b64_tr_b16 v[234:235], v35 offset:2464\n"
      "ds_read_b64_tr_b16 v[236:237], v35 offset:9376\n"
      "ds_read_b64_tr_b16 v[238:239], v35 offset:11680\n"
      "ds_read_b64_tr_b16 v[240:241], v35 offset:37024\n"
      "ds_read_b64_tr_b16 v[242:243], v35 offset:39328\n"
      "ds_read_b64_tr_b16 v[244:245], v35 offset:46240\n"
      "ds_read_b64_tr_b16 v[246:247], v35 offset:48544\n"
      "s_waitcnt lgkmcnt(0)\n"
      "ds_read_b64_tr_b16 v[208:209], v37 offset:0\n"
      "ds_read_b64_tr_b16 v[210:211], v37 offset:2304\n"
      "ds_read_b64_tr_b16 v[212:213], v37 offset:9216\n"
      "ds_read_b64_tr_b16 v[214:215], v37 offset:11520\n"
      "s_waitcnt lgkmcnt(0)\n"
      "s_barrier\n"
      "s_mov_b32 s30, 4710656\n"
      "s_mul_i32 s44, s34, 256\n"
      "s_add_u32 s28, s74, s44\n"
      "s_addc_u32 s29, s75, 0\n"
      "s_and_b32 s29, s29, 0xffff\n"
      "s_mul_i32 s45, s57, 18\n"
      "s_add_u32 s45, s45, 48\n"
      "s_lshl_b32 s44, s45, 15\n"
      "s_lshl_b32 s45, s45, 10\n"
      "s_add_u32 m0, s45, 24576\n"
      "s_nop 0\n"
      "buffer_load_dwordx4 v28, s[28:31], s44 offen lds\n"
      "s_add_u32 s44, s44, 32768\n"
      "s_add_u32 m0, m0, 1024\n"
      "s_nop 0\n"
      "buffer_load_dwordx4 v29, s[28:31], s44 offen lds\n"
      "s_add_u32 s44, s44, 32768\n"
      "s_add_u32 m0, m0, 1024\n"
      "s_nop 0\n"
      "buffer_load_dwordx4 v28, s[28:31], s44 offen lds\n"
      "s_add_u32 s44, s44, 32768\n"
      "s_add_u32 m0, m0, 1024\n"
      "s_nop 0\n"
      "buffer_load_dwordx4 v29, s[28:31], s44 offen lds\n"
      "s_add_u32 s44, s44, 32768\n"
      "s_add_u32 m0, m0, 1024\n"
      "s_nop 0\n"
      "buffer_load_dwordx4 v28, s[28:31], s44 offen lds\n"
      "s_add_u32 s44, s44, 32768\n"
      "s_add_u32 m0, m0, 1024\n"
      "s_nop 0\n"
      "buffer_load_dwordx4 v29, s[28:31], s44 offen lds\n"
      "s_add_u32 s44, s44, 32768\n"
      "s_add_u32 m0, m0, 1024\n"
      "s_nop 0\n"
      "buffer_load_dwordx4 v28, s[28:31], s44 offen lds\n"
      "s_add_u32 s44, s44, 32768\n"
      "s_add_u32 m0, m0, 1024\n"
      "s_nop 0\n"
      "buffer_load_dwordx4 v29, s[28:31], s44 offen lds\n"
      "s_add_u32 s44, s44, 32768\n"
      "s_add_u32 m0, m0, 1024\n"
      "s_nop 0\n"
      "buffer_load_dwordx4 v28, s[28:31], s44 offen lds\n"
      "s_add_u32 s44, s44, 32768\n"
      "s_add_u32 m0, m0, 1024\n"
      "s_nop 0\n"
      "buffer_load_dwordx4 v29, s[28:31], s44 offen lds\n"
      "s_add_u32 s44, s44, 32768\n"
      "s_add_u32 m0, m0, 1024\n"
      "s_nop 0\n"
      "buffer_load_dwordx4 v28, s[28:31], s44 offen lds\n"
      "s_add_u32 s44, s44, 32768\n"
      "s_add_u32 m0, m0, 1024\n"
      "s_nop 0\n"
      "buffer_load_dwordx4 v29, s[28:31], s44 offen lds\n"
      "s_add_u32 s44, s44, 32768\n"
      "s_add_u32 m0, m0, 1024\n"
      "s_nop 0\n"
      "buffer_load_dwordx4 v28, s[28:31], s44 offen lds\n"
      "s_add_u32 s44, s44, 32768\n"
      "s_add_u32 m0, m0, 1024\n"
      "s_nop 0\n"
      "buffer_load_dwordx4 v29, s[28:31], s44 offen lds\n"
      "s_add_u32 s44, s44, 32768\n"
      "s_add_u32 m0, m0, 1024\n"
      "s_nop 0\n"
      "buffer_load_dwordx4 v28, s[28:31], s44 offen lds\n"
      "s_add_u32 s44, s44, 32768\n"
      "s_add_u32 m0, m0, 1024\n"
      "s_nop 0\n"
      "buffer_load_dwordx4 v29, s[28:31], s44 offen lds\n"
      "s_add_u32 s44, s44, 32768\n"
      "s_add_u32 m0, m0, 1024\n"
      "s_nop 0\n"
      "buffer_load_dwordx4 v28, s[28:31], s44 offen lds\n"
      "s_add_u32 s44, s44, 32768\n"
      "s_add_u32 m0, m0, 1024\n"
      "s_nop 0\n"
      "buffer_load_dwordx4 v29, s[28:31], s44 offen lds\n"
      "v_mov_b32 v7, v32\n"
      "s_waitcnt vmcnt(0)\n"
      "s_barrier\n"
      "v_add_u32 v62, 24576, v7\n"
      "ds_read_b64_tr_b16 v[8:9], v62 offset:0\n"
      "ds_read_b64_tr_b16 v[10:11], v62 offset:512\n"
      "ds_read_b64_tr_b16 v[12:13], v62 offset:4096\n"
      "ds_read_b64_tr_b16 v[14:15], v62 offset:4608\n"
      "v_add_u32 v62, 32768, v7\n"
      "ds_read_b64_tr_b16 v[16:17], v62 offset:0\n"
      "ds_read_b64_tr_b16 v[18:19], v62 offset:512\n"
      "ds_read_b64_tr_b16 v[20:21], v62 offset:4096\n"
      "ds_read_b64_tr_b16 v[22:23], v62 offset:4608\n"
      "s_waitcnt lgkmcnt(0)\n"
      "v_add_u32 v62, 40960, v7\n"
      "ds_read_b64_tr_b16 v[24:25], v62 offset:0\n"
      "ds_read_b64_tr_b16 v[26:27], v62 offset:512\n"
      "ds_read_b64_tr_b16 v[28:29], v62 offset:4096\n"
      "ds_read_b64_tr_b16 v[30:31], v62 offset:4608\n"
      "v_add_u32 v62, 49152, v7\n"
      "ds_read_b64_tr_b16 v[32:33], v62 offset:0\n"
      "ds_read_b64_tr_b16 v[34:35], v62 offset:512\n"
      "ds_read_b64_tr_b16 v[36:37], v62 offset:4096\n"
      "ds_read_b64_tr_b16 v[38:39], v62 offset:4608\n"
      "s_waitcnt lgkmcnt(0)\n"
      "v_add_u32 v62, 57344, v7\n"
      "ds_read_b64_tr_b16 v[40:41], v62 offset:0\n"
      "ds_read_b64_tr_b16 v[42:43], v62 offset:512\n"
      "ds_read_b64_tr_b16 v[44:45], v62 offset:4096\n"
      "ds_read_b64_tr_b16 v[46:47], v62 offset:4608\n"
      "v_add_u32 v62, 65536, v7\n"
      "ds_read_b64_tr_b16 v[48:49], v62 offset:0\n"
      "ds_read_b64_tr_b16 v[50:51], v62 offset:512\n"
      "ds_read_b64_tr_b16 v[52:53], v62 offset:4096\n"
      "ds_read_b64_tr_b16 v[54:55], v62 offset:4608\n"
      "s_waitcnt lgkmcnt(0)\n"
      "s_barrier\n"
      "ds_write_b128 v5, v[8:11] offset:0\n"
      "ds_write_b128 v5, v[12:15] offset:1024\n"
      "ds_write_b128 v5, v[16:19] offset:2048\n"
      "ds_write_b128 v5, v[20:23] offset:3072\n"
      "ds_write_b128 v5, v[24:27] offset:16384\n"
      "ds_write_b128 v5, v[28:31] offset:17408\n"
      "ds_write_b128 v5, v[32:35] offset:18432\n"
      "ds_write_b128 v5, v[36:39] offset:19456\n"
      "ds_write_b128 v5, v[40:43] offset:32768\n"
      "ds_write_b128 v5, v[44:47] offset:33792\n"
      "ds_write_b128 v5, v[48:51] offset:34816\n"
      "ds_write_b128 v5, v[52:55] offset:35840\n"
      "s_waitcnt lgkmcnt(0)\n"
      "v_add_u32 v62, 73728, v7\n"
      "ds_read_b64_tr_b16 v[8:9], v62 offset:0\n"
      "ds_read_b64_tr_b16 v[10:11], v62 offset:512\n"
      "ds_read_b64_tr_b16 v[12:13], v62 offset:4096\n"
      "ds_read_b64_tr_b16 v[14:15], v62 offset:4608\n"
      "v_add_u32 v62, 81920, v7\n"
      "ds_read_b64_tr_b16 v[16:17], v62 offset:0\n"
      "ds_read_b64_tr_b16 v[18:19], v62 offset:512\n"
      "ds_read_b64_tr_b16 v[20:21], v62 offset:4096\n"
      "ds_read_b64_tr_b16 v[22:23], v62 offset:4608\n"
      "s_waitcnt lgkmcnt(0)\n"
      "v_add_u32 v62, 90112, v7\n"
      "ds_read_b64_tr_b16 v[24:25], v62 offset:0\n"
      "ds_read_b64_tr_b16 v[26:27], v62 offset:512\n"
      "ds_read_b64_tr_b16 v[28:29], v62 offset:4096\n"
      "ds_read_b64_tr_b16 v[30:31], v62 offset:4608\n"
      "v_add_u32 v62, 98304, v7\n"
      "ds_read_b64_tr_b16 v[32:33], v62 offset:0\n"
      "ds_read_b64_tr_b16 v[34:35], v62 offset:512\n"
      "ds_read_b64_tr_b16 v[36:37], v62 offset:4096\n"
      "ds_read_b64_tr_b16 v[38:39], v62 offset:4608\n"
      "s_waitcnt lgkmcnt(0)\n"
      "v_add_u32 v62, 106496, v7\n"
      "ds_read_b64_tr_b16 v[40:41], v62 offset:0\n"
      "ds_read_b64_tr_b16 v[42:43], v62 offset:512\n"
      "ds_read_b64_tr_b16 v[44:45], v62 offset:4096\n"
      "ds_read_b64_tr_b16 v[46:47], v62 offset:4608\n"
      "v_add_u32 v62, 114688, v7\n"
      "ds_read_b64_tr_b16 v[48:49], v62 offset:0\n"
      "ds_read_b64_tr_b16 v[50:51], v62 offset:512\n"
      "ds_read_b64_tr_b16 v[52:53], v62 offset:4096\n"
      "ds_read_b64_tr_b16 v[54:55], v62 offset:4608\n"
      "s_waitcnt lgkmcnt(0)\n"
      "s_barrier\n"
      "ds_write_b128 v5, v[8:11] offset:49152\n"
      "ds_write_b128 v5, v[12:15] offset:50176\n"
      "ds_write_b128 v5, v[16:19] offset:51200\n"
      "ds_write_b128 v5, v[20:23] offset:52224\n"
      "v_add_u32 v62, 65536, v5\n"
      "ds_write_b128 v62, v[24:27] offset:0\n"
      "v_add_u32 v62, 65536, v5\n"
      "ds_write_b128 v62, v[28:31] offset:1024\n"
      "v_add_u32 v62, 65536, v5\n"
      "ds_write_b128 v62, v[32:35] offset:2048\n"
      "v_add_u32 v62, 65536, v5\n"
      "ds_write_b128 v62, v[36:39] offset:3072\n"
      "v_add_u32 v62, 65536, v5\n"
      "ds_write_b128 v62, v[40:43] offset:16384\n"
      "v_add_u32 v62, 65536, v5\n"
      "ds_write_b128 v62, v[44:47] offset:17408\n"
      "v_add_u32 v62, 65536, v5\n"
      "ds_write_b128 v62, v[48:51] offset:18432\n"
      "v_add_u32 v62, 65536, v5\n"
      "ds_write_b128 v62, v[52:55] offset:19456\n"
      "s_waitcnt lgkmcnt(0)\n"
      "v_add_u32 v62, 122880, v7\n"
      "ds_read_b64_tr_b16 v[8:9], v62 offset:0\n"
      "ds_read_b64_tr_b16 v[10:11], v62 offset:512\n"
      "ds_read_b64_tr_b16 v[12:13], v62 offset:4096\n"
      "ds_read_b64_tr_b16 v[14:15], v62 offset:4608\n"
      "v_add_u32 v62, 131072, v7\n"
      "ds_read_b64_tr_b16 v[16:17], v62 offset:0\n"
      "ds_read_b64_tr_b16 v[18:19], v62 offset:512\n"
      "ds_read_b64_tr_b16 v[20:21], v62 offset:4096\n"
      "ds_read_b64_tr_b16 v[22:23], v62 offset:4608\n"
      "s_waitcnt lgkmcnt(0)\n"
      "v_add_u32 v62, 139264, v7\n"
      "ds_read_b64_tr_b16 v[24:25], v62 offset:0\n"
      "ds_read_b64_tr_b16 v[26:27], v62 offset:512\n"
      "ds_read_b64_tr_b16 v[28:29], v62 offset:4096\n"
      "ds_read_b64_tr_b16 v[30:31], v62 offset:4608\n"
      "v_add_u32 v62, 147456, v7\n"
      "ds_read_b64_tr_b16 v[32:33], v62 offset:0\n"
      "ds_read_b64_tr_b16 v[34:35], v62 offset:512\n"
      "ds_read_b64_tr_b16 v[36:37], v62 offset:4096\n"
      "ds_read_b64_tr_b16 v[38:39], v62 offset:4608\n"
      "s_waitcnt lgkmcnt(0)\n"
      "v_add_u32 v62, 155648, v7\n"
      "ds_read_b64_tr_b16 v[40:41], v62 offset:0\n"
      "ds_read_b64_tr_b16 v[42:43], v62 offset:512\n"
      "ds_read_b64_tr_b16 v[44:45], v62 offset:4096\n"
      "ds_read_b64_tr_b16 v[46:47], v62 offset:4608\n"
      "v_add_u32 v62, 13568, v7\n"
      "ds_read_b64_tr_b16 v[248:249], v62 offset:0\n"
      "ds_read_b64_tr_b16 v[250:251], v62 offset:512\n"
      "ds_read_b64_tr_b16 v[252:253], v62 offset:4096\n"
      "ds_read_b64_tr_b16 v[254:255], v62 offset:4608\n"
      "s_waitcnt lgkmcnt(0)\n"
      "s_barrier\n"
      "s_lshl_b32 s44, s57, 11\n"
      "s_sub_u32 s44, 98304, s44\n"
      "v_add_u32 v62, s44, v5\n"
      "ds_write_b128 v62, v[8:11] offset:0\n"
      "ds_write_b128 v62, v[12:15] offset:1024\n"
      "ds_write_b128 v62, v[216:219] offset:8192\n"
      "ds_write_b128 v62, v[220:223] offset:9216\n"
      "ds_write_b128 v62, v[224:227] offset:16384\n"
      "ds_write_b128 v62, v[228:231] offset:17408\n"
      "ds_write_b128 v62, v[232:235] offset:24576\n"
      "ds_write_b128 v62, v[236:239] offset:25600\n"
      "ds_write_b128 v62, v[240:243] offset:32768\n"
      "ds_write_b128 v62, v[244:247] offset:33792\n"
      "s_waitcnt lgkmcnt(0)\n"
      "v_mov_b64 v[216:217], v[16:17]\n"
      "v_mov_b64 v[218:219], v[18:19]\n"
      "v_mov_b64 v[220:221], v[20:21]\n"
      "v_mov_b64 v[222:223], v[22:23]\n"
      "v_mov_b64 v[224:225], v[24:25]\n"
      "v_mov_b64 v[226:227], v[26:27]\n"
      "v_mov_b64 v[228:229], v[28:29]\n"
      "v_mov_b64 v[230:231], v[30:31]\n"
      "v_mov_b64 v[232:233], v[32:33]\n"
      "v_mov_b64 v[234:235], v[34:35]\n"
      "v_mov_b64 v[236:237], v[36:37]\n"
      "v_mov_b64 v[238:239], v[38:39]\n"
      "v_mov_b64 v[240:241], v[40:41]\n"
      "v_mov_b64 v[242:243], v[42:43]\n"
      "v_mov_b64 v[244:245], v[44:45]\n"
      "v_mov_b64 v[246:247], v[46:47]\n"
      "s_barrier\n"
      "v_mbcnt_lo_u32_b32 v16, -1, 0\n"
      "v_mbcnt_hi_u32_b32 v16, -1, v16\n"
      "ds_write_b128 v60, v[64:67]\n"
      "ds_write_b128 v61, v[68:71]\n"
      "ds_write_b128 v63, v[56:59]\n"
      "v_and_b32 v17, 1, v16\n"
      "v_lshlrev_b32 v17, 4, v17\n"
      "v_mov_b32 v18, 0x3f80\n"
      "v_lshlrev_b32 v18, v17, v18\n"
      "v_lshrrev_b32 v17, 1, v16\n"
      "v_mov_b32 v19, 0\n"
      "v_cmp_eq_u32 vcc, 0, v17\n"
      "s_nop 1\n"
      "v_cndmask_b32 v20, v19, v18, vcc\n"
      "v_cmp_eq_u32 vcc, 1, v17\n"
      "s_nop 1\n"
      "v_cndmask_b32 v21, v19, v18, vcc\n"
      "v_cmp_eq_u32 vcc, 2, v17\n"
      "s_nop 1\n"
      "v_cndmask_b32 v22, v19, v18, vcc\n"
      "v_cmp_eq_u32 vcc, 3, v17\n"
      "s_nop 1\n"
      "v_cndmask_b32 v23, v19, v18, vcc\n"
      "v_add_u32 v17, 21760, v0\n"
      "s_mov_b32 exec_lo, 0xff\n"
      "s_mov_b32 exec_hi, 0\n"
      "ds_write_b128 v17, v[20:23]\n"
      "s_mov_b64 exec, -1\n"
      "s_lshl_b32 s44, s58, 8\n"
      "s_add_u32 s44, s44, 0\n"
      "s_lshl_b32 s45, s58, 6\n"
      "s_add_u32 s45, s45, 512\n"
      "s_lshl_b32 s46, s58, 5\n"
      "s_add_u32 s46, s46, s34\n"
      "s_mul_i32 s46, s46, 256\n"
      "s_add_u32 s46, s46, 1024\n"
      "L_start_%=:\n"
      "buffer_load_dword v20, off, s[24:27], s44 sc1\n"
      "buffer_load_dword v21, off, s[24:27], s45 sc1\n"
      "buffer_load_dword v22, off, s[24:27], s46 sc1\n"
      "s_waitcnt vmcnt(0)\n"
      "v_readfirstlane_b32 s47, v20\n"
      "v_readfirstlane_b32 s48, v21\n"
      "v_readfirstlane_b32 s41, v22\n"
      "s_nop 1\n"
      "s_cmp_ge_u32 s47, 32\n"
      "s_cselect_b32 s49, 1, 0\n"
      "s_cmp_lg_u32 s48, 0\n"
      "s_cselect_b32 s49, s49, 0\n"
      "s_cmp_lg_u32 s41, 0\n"
      "s_cselect_b32 s49, s49, 0\n"
      "s_cmp_lg_u32 s49, 0\n"
      "s_cbranch_scc1 L_startok_%=\n"
      "s_sub_u32 s42, s42, 1\n"
      "s_cbranch_scc1 L_abort_%=\n"
      "s_sleep 2\n"
      "s_branch L_start_%=\n"
      "L_startok_%=:\n"
      "s_sub_u32 s48, s48, 1\n"
      "s_and_b32 s48, s48, 1\n"
      "s_lshl_b32 s48, s48, 13\n"
      "s_add_u32 s36, s36, s48\n"
      "s_cmp_ge_u32 s57, 2\n"
      "s_cbranch_scc1 L_nokt0_%=\n"
      "s_lshl_b32 s44, s57, 12\n"
      "s_add_u32 s45, s44, s39\n"
      "s_add_u32 m0, s44, 13568\n"
      "s_nop 0\n"
      "buffer_load_dwordx4 v0, s[24:27], s45 offen offset:0 sc1 lds\n"
      "buffer_load_dwordx4 v0, s[24:27], s45 offen offset:1024 sc1 lds\n"
      "buffer_load_dwordx4 v0, s[24:27], s45 offen offset:2048 sc1 lds\n"
      "buffer_load_dwordx4 v0, s[24:27], s45 offen offset:3072 sc1 lds\n"
      "L_nokt0_%=:\n"
      "s_mov_b32 s35, 0\n"
      "s_mov_b32 s50, 0\n"
      "s_mov_b32 s51, 1179648\n"
      "s_mov_b32 s52, 32768\n"
      "s_mov_b32 s53, 0\n"
      "s_mov_b32 s62, -1\n"
      "s_mov_b32 s63, 0\n"
      "s_mov_b32 s61, 0xffffff7f\n"
      "s_mov_b32 s98, 16\n"
      "s_mov_b32 s99, 16\n"
      "s_mov_b32 s94, 0\n"
      "s_mov_b32 s95, 0\n"
      "s_lshl_b32 s44, s57, 11\n"
      "s_sub_u32 s45, 106496, s44\n"
      "v_add_u32 v7, s45, v5\n"
      "s_add_u32 s67, s44, 8192\n"
      "v_mov_b64 v[16:17], 0\n"
      "v_mov_b64 v[18:19], 0\n"
      "v_mov_b64 v[20:21], 0\n"
      "v_mov_b64 v[22:23], 0\n"
      "v_mov_b64 v[24:25], 0\n"
      "v_mov_b64 v[26:27], 0\n"
      "ds_read_b128 v[40:43], v5 offset:0\n"
      "ds_read_b128 v[44:47], v5 offset:1024\n"
      "ds_read_b128 v[48:51], v5 offset:2048\n"
      "ds_read_b128 v[52:55], v5 offset:3072\n"
      "ds_read_b128 v[56:59], v5 offset:16384\n"
      "ds_read_b128 v[60:63], v5 offset:17408\n"
      "ds_read_b128 v[8:11], v5 offset:18432\n"
      "ds_read_b128 v[12:15], v5 offset:19456\n"
      "s_waitcnt vmcnt(0) lgkmcnt(0)\n"
      "s_barrier\n"
      "L_loop_%=:\n"
      "ds_read_b128 v[28:31], v2 offset:0\n"
      "ds_read_b128 v[32:35], v2 offset:64\n"
      "ds_read_b128 v[36:39], v2 offset:128\n"
      "v_add_u32 v68, 0x10000, v5\n"
      "s_lshl_b32 s44, s57, 11\n"
      "s_sub_u32 s44, 98304, s44\n"
      "v_add_u32 v69, s44, v5\n"
      "s_waitcnt lgkmcnt(2)\n"
      "v_smfmac_f32_16x16x64_bf16 v[24:27], v[28:31], v[40:47], v1\n"
      "v_smfmac_f32_16x16x64_bf16 v[16:19], v[28:31], a[0:7], v1\n"
      "ds_read_b128 v[40:43], v5 offset:32768\n"
      "ds_read_b128 v[44:47], v5 offset:33792\n"
      "v_smfmac_f32_16x16x64_bf16 v[20:23], v[28:31], a[144:151], v1\n"
      "ds_read_b128 v[64:67], v2 offset:832\n"
      "s_waitcnt lgkmcnt(4)\n"
      "v_smfmac_f32_16x16x64_bf16 v[24:27], v[32:35], v[48:55], v1\n"
      "v_smfmac_f32_16x16x64_bf16 v[16:19], v[32:35], a[8:15], v1\n"
      "ds_read_b128 v[48:51], v5 offset:34816\n"
      "ds_read_b128 v[52:55], v5 offset:35840\n"
      "v_smfmac_f32_16x16x64_bf16 v[20:23], v[32:35], a[152:159], v1\n"
      "ds_read_b128 v[28:31], v2 offset:192\n"
      "s_waitcnt lgkmcnt(6)\n"
      "v_smfmac_f32_16x16x64_bf16 v[24:27], v[36:39], v[56:63], v1\n"
      "v_smfmac_f32_16x16x64_bf16 v[16:19], v[36:39], a[16:23], v1\n"
      "ds_read_b128 v[56:59], v5 offset:49152\n"
      "ds_read_b128 v[60:63], v5 offset:50176\n"
      "v_smfmac_f32_16x16x64_bf16 v[20:23], v[36:39], a[160:167], v1\n"
      "ds_read_b128 v[32:35], v2 offset:256\n"
      "s_waitcnt lgkmcnt(6)\n"
      "v_smfmac_f32_16x16x64_bf16 v[24:27], v[64:67], v[216:223], v1\n"
      "v_smfmac_f32_16x16x64_bf16 v[16:19], v[64:67], a[104:111], v1\n"
      "v_smfmac_f32_16x16x64_bf16 v[20:23], v[64:67], a[248:255], v1\n"
      "ds_read_b128 v[36:39], v2 offset:896\n"
      "s_waitcnt lgkmcnt(4)\n"
      "v_smfmac_f32_16x16x64_bf16 v[24:27], v[28:31], v[8:15], v1\n"
      "v_smfmac_f32_16x16x64_bf16 v[16:19], v[28:31], a[24:31], v1\n"
      "ds_read_b128 v[8:11], v5 offset:51200\n"
      "ds_read_b128 v[12:15], v5 offset:52224\n"
      "v_smfmac_f32_16x16x64_bf16 v[20:23], v[28:31], a[168:175], v1\n"
      "ds_read_b128 v[64:67], v2 offset:320\n"
      "s_waitcnt lgkmcnt(4)\n"
      "v_smfmac_f32_16x16x64_bf16 v[24:27], v[32:35], v[40:47], v1\n"
      "v_smfmac_f32_16x16x64_bf16 v[16:19], v[32:35], a[32:39], v1\n"
      "ds_read_b128 v[40:43], v68 offset:0\n"
      "ds_read_b128 v[44:47], v68 offset:1024\n"
      "v_smfmac_f32_16x16x64_bf16 v[20:23], v[32:35], a[176:183], v1\n"
      "ds_read_b128 v[28:31], v2 offset:384\n"
      "s_waitcnt lgkmcnt(6)\n"
      "v_smfmac_f32_16x16x64_bf16 v[24:27], v[36:39], v[224:231], v1\n"
      "v_smfmac_f32_16x16x64_bf16 v[16:19], v[36:39], a[112:119], v1\n"
      "v_smfmac_f32_16x16x64_bf16 v[20:23], v[36:39], v[72:79], v1\n"
      "ds_read_b128 v[32:35], v2 offset:960\n"
      "s_waitcnt lgkmcnt(4)\n"
      "v_smfmac_f32_16x16x64_bf16 v[24:27], v[64:67], v[48:55], v1\n"
      "v_smfmac_f32_16x16x64_bf16 v[16:19], v[64:67], a[40:47], v1\n"
      "ds_read_b128 v[48:51], v68 offset:2048\n"
      "ds_read_b128 v[52:55], v68 offset:3072\n"
      "v_smfmac_f32_16x16x64_bf16 v[20:23], v[64:67], a[184:191], v1\n"
      "ds_read_b128 v[36:39], v2 offset:448\n"
      "s_waitcnt lgkmcnt(4)\n"
      "v_smfmac_f32_16x16x64_bf16 v[24:27], v[28:31], v[56:63], v1\n"
      "v_smfmac_f32_16x16x64_bf16 v[16:19], v[28:31], a[48:55], v1\n"
      "ds_read_b128 v[56:59], v68 offset:16384\n"
      "ds_read_b128 v[60:63], v68 offset:17408\n"
      "v_smfmac_f32_16x16x64_bf16 v[20:23], v[28:31], a[192:199], v1\n"
      "ds_read_b128 v[64:67], v2 offset:512\n"
      "s_waitcnt lgkmcnt(6)\n"
      "v_smfmac_f32_16x16x64_bf16 v[24:27], v[32:35], v[232:239], v1\n"
      "v_smfmac_f32_16x16x64_bf16 v[16:19], v[32:35], a[120:127], v1\n"
      "v_smfmac_f32_16x16x64_bf16 v[20:23], v[32:35], v[80:87], v1\n"
      "ds_read_b128 v[28:31], v2 offset:1024\n"
      "s_waitcnt lgkmcnt(4)\n"
      "v_smfmac_f32_16x16x64_bf16 v[24:27], v[36:39], v[8:15], v1\n"
      "v_smfmac_f32_16x16x64_bf16 v[16:19], v[36:39], a[56:63], v1\n"
      "ds_read_b128 v[8:11], v68 offset:18432\n"
      "ds_read_b128 v[12:15], v68 offset:19456\n"
      "v_smfmac_f32_16x16x64_bf16 v[20:23], v[36:39], a[200:207], v1\n"
      "ds_read_b128 v[32:35], v2 offset:576\n"
      "s_waitcnt lgkmcnt(4)\n"
      "v_smfmac_f32_16x16x64_bf16 v[24:27], v[64:67], v[40:47], v1\n"
      "v_smfmac_f32_16x16x64_bf16 v[16:19], v[64:67], a[64:71], v1\n"
      "ds_read_b128 v[40:43], v69 offset:0\n"
      "ds_read_b128 v[44:47], v69 offset:1024\n"
      "v_smfmac_f32_16x16x64_bf16 v[20:23], v[64:67], a[208:215], v1\n"
      "ds_read_b128 v[36:39], v2 offset:640\n"
      "s_waitcnt lgkmcnt(6)\n"
      "v_smfmac_f32_16x16x64_bf16 v[24:27], v[28:31], v[240:247], v1\n"
      "v_smfmac_f32_16x16x64_bf16 v[16:19], v[28:31], a[128:135], v1\n"
      "v_smfmac_f32_16x16x64_bf16 v[20:23], v[28:31], v[88:95], v1\n"
      "ds_read_b128 v[64:67], v2 offset:704\n"
      "s_waitcnt lgkmcnt(4)\n"
      "v_smfmac_f32_16x16x64_bf16 v[24:27], v[32:35], v[48:55], v1\n"
      "v_smfmac_f32_16x16x64_bf16 v[16:19], v[32:35], a[72:79], v1\n"
      "v_smfmac_f32_16x16x64_bf16 v[20:23], v[32:35], a[216:223], v1\n"
      "ds_read_b128 v[28:31], v2 offset:768\n"
      "s_waitcnt lgkmcnt(2)\n"
      "v_smfmac_f32_16x16x64_bf16 v[24:27], v[36:39], v[56:63], v1\n"
      "v_smfmac_f32_16x16x64_bf16 v[16:19], v[36:39], a[80:87], v1\n"
      "v_smfmac_f32_16x16x64_bf16 v[20:23], v[36:39], a[224:231], v1\n"
      "ds_read_b128 v[32:35], v2 offset:1088\n"
      "s_waitcnt lgkmcnt(2)\n"
      "v_smfmac_f32_16x16x64_bf16 v[24:27], v[64:67], v[8:15], v1\n"
      "v_smfmac_f32_16x16x64_bf16 v[16:19], v[64:67], a[88:95], v1\n"
      "v_smfmac_f32_16x16x64_bf16 v[20:23], v[64:67], a[232:239], v1\n"
      "s_add_u32 s44, s39, s67\n"
      "buffer_load_dwordx4 v[8:11], v0, s[24:27], s44 offen sc1\n"
      "buffer_load_dwordx4 v[12:15], v0, s[24:27], s44 offen offset:1024 sc1\n"
      "s_waitcnt lgkmcnt(1)\n"
      "v_smfmac_f32_16x16x64_bf16 v[16:19], v[28:31], a[96:103], v1\n"
      "v_smfmac_f32_16x16x64_bf16 v[20:23], v[28:31], a[240:247], v1\n"
      "s_waitcnt lgkmcnt(0)\n"
      "v_smfmac_f32_16x16x64_bf16 v[16:19], v[32:35], a[136:143], v1\n"
      "v_smfmac_f32_16x16x64_bf16 v[20:23], v[32:35], v[96:103], v1\n"
      "v_smfmac_f32_16x16x64_bf16 v[24:27], v[28:31], v[40:47], v1\n"
      "ds_read_b128 v[48:51], v0 offset:13568\n"
      "ds_read_b128 v[52:55], v0 offset:17664\n"
      "ds_read_b128 v[56:59], v0 offset:14592\n"
      "ds_read_b128 v[60:63], v0 offset:18688\n"
      "v_smfmac_f32_16x16x64_bf16 v[24:27], v[32:35], v[248:255], v1\n"
      "s_nop 0\n"
      "v_cvt_pk_bf16_f32 v16, v16, v17\n"
      "v_cvt_pk_bf16_f32 v17, v18, v19\n"
      "v_cvt_pk_bf16_f32 v20, v20, v21\n"
      "v_cvt_pk_bf16_f32 v21, v22, v23\n"
      "ds_write_b64 v3, v[16:17] offset:9472\n"
      "ds_write_b64 v3, v[20:21] offset:11520\n"
      "s_nop 0\n"
      "v_cvt_pk_bf16_f32 v24, v24, v25\n"
      "v_cvt_pk_bf16_f32 v25, v26, v27\n"
      "s_cmp_eq_u32 s35, 63\n"
      "s_cbranch_scc1 L_final_%=\n"
      "s_waitcnt lgkmcnt(0)\n"
      "s_barrier\n"
      "ds_read_b64_tr_b16 v[28:29], v4 offset:9472\n"
      "ds_read_b64_tr_b16 v[30:31], v4 offset:9728\n"
      "ds_read_b64_tr_b16 v[32:33], v4 offset:11520\n"
      "ds_read_b64_tr_b16 v[34:35], v4 offset:11776\n"
      "ds_read_b64_tr_b16 v[40:41], v4 offset:9984\n"
      "ds_read_b64_tr_b16 v[42:43], v4 offset:10240\n"
      "ds_read_b64_tr_b16 v[44:45], v4 offset:12032\n"
      "ds_read_b64_tr_b16 v[46:47], v4 offset:12288\n"
      "ds_read_b64_tr_b16 v[64:65], v4 offset:10496\n"
      "ds_read_b64_tr_b16 v[66:67], v4 offset:10752\n"
      "ds_read_b64_tr_b16 v[68:69], v4 offset:12544\n"
      "ds_read_b64_tr_b16 v[70:71], v4 offset:12800\n"
      "s_waitcnt lgkmcnt(8)\n"
      "v_mfma_f32_16x16x32_bf16 v[20:23], v[48:51], v[28:31], 0\n"
      "v_mfma_f32_16x16x32_bf16 v[36:39], v[52:55], v[28:31], 0\n"
      "v_mfma_f32_16x16x32_bf16 v[16:19], v[32:35], v[28:31], 0\n"
      "ds_read_b128 v[48:51], v0 offset:15616\n"
      "ds_read_b128 v[52:55], v0 offset:19712\n"
      "s_waitcnt lgkmcnt(6)\n"
      "v_mfma_f32_16x16x32_bf16 v[20:23], v[56:59], v[40:43], v[20:23]\n"
      "v_mfma_f32_16x16x32_bf16 v[36:39], v[60:63], v[40:43], v[36:39]\n"
      "v_mfma_f32_16x16x32_bf16 v[16:19], v[44:47], v[40:43], v[16:19]\n"
      "ds_read_b64_tr_b16 v[28:29], v4 offset:11008\n"
      "ds_read_b64_tr_b16 v[30:31], v4 offset:11264\n"
      "ds_read_b64_tr_b16 v[32:33], v4 offset:13056\n"
      "ds_read_b64_tr_b16 v[34:35], v4 offset:13312\n"
      "ds_read_b128 v[56:59], v0 offset:16640\n"
      "ds_read_b128 v[60:63], v0 offset:20736\n"
      "s_waitcnt lgkmcnt(6)\n"
      "v_mfma_f32_16x16x32_bf16 v[20:23], v[48:51], v[64:67], v[20:23]\n"
      "v_mfma_f32_16x16x32_bf16 v[36:39], v[52:55], v[64:67], v[36:39]\n"
      "v_mfma_f32_16x16x32_bf16 v[16:19], v[68:71], v[64:67], v[16:19]\n"
      "v_mov_b32 v44, 0x3f803f80\n"
      "v_mov_b32 v45, 0x3f803f80\n"
      "v_mov_b32 v46, 0x3f803f80\n"
      "v_mov_b32 v47, 0x3f803f80\n"
      "s_waitcnt lgkmcnt(0)\n"
      "v_mfma_f32_16x16x32_bf16 v[20:23], v[56:59], v[28:31], v[20:23]\n"
      "v_mfma_f32_16x16x32_bf16 v[36:39], v[60:63], v[28:31], v[36:39]\n"
      "v_mfma_f32_16x16x32_bf16 v[16:19], v[32:35], v[28:31], v[16:19]\n"
      "v_mov_b64 v[48:49], 0\n"
      "v_mov_b64 v[50:51], 0\n"
      "v_mov_b64 v[52:53], 0\n"
      "v_mov_b64 v[54:55], 0\n"
      "v_mov_b64 v[56:57], 0\n"
      "v_mov_b64 v[58:59], 0\n"
      "v_mov_b64 v[60:61], 0\n"
      "v_mov_b64 v[62:63], 0\n"
      "v_max3_f32 v28, v20, v21, v22\n"
      "v_max3_f32 v29, v23, v36, v37\n"
      "v_max3_f32 v30, v38, v39, v16\n"
      "v_max3_f32 v31, v17, v18, v19\n"
      "v_max3_f32 v28, v28, v29, v30\n"
      "v_max_f32 v28, v28, v31\n"
      "v_mov_b32 v29, v28\n"
      "v_mov_b64 v[64:65], 0\n"
      "v_mov_b64 v[66:67], 0\n"
      "v_permlane16_swap_b32 v28, v29\n"
      "v_max_f32 v28, v28, v29\n"
      "v_mov_b32 v29, v28\n"
      "s_nop 1\n"
      "v_permlane32_swap_b32 v28, v29\n"
      "v_max_f32 v28, v28, v29\n"
      "v_mul_f32_e64 v28, -s55, v28\n"
      "v_pk_fma_f32 v[16:17], v[16:17], s[96:97], v[28:29] op_sel_hi:[1,1,0]\n"
      "v_pk_fma_f32 v[18:19], v[18:19], s[96:97], v[28:29] op_sel_hi:[1,1,0]\n"
      "v_pk_fma_f32 v[20:21], v[20:21], s[96:97], v[28:29] op_sel_hi:[1,1,0]\n"
      "v_pk_fma_f32 v[22:23], v[22:23], s[96:97], v[28:29] op_sel_hi:[1,1,0]\n"
      "v_pk_fma_f32 v[36:37], v[36:37], s[96:97], v[28:29] op_sel_hi:[1,1,0]\n"
      "v_pk_fma_f32 v[38:39], v[38:39], s[96:97], v[28:29] op_sel_hi:[1,1,0]\n"
      "v_exp_f32 v16, v16\n"
      "v_exp_f32 v17, v17\n"
      "v_exp_f32 v18, v18\n"
      "v_exp_f32 v19, v19\n"
      "v_exp_f32 v20, v20\n"
      "v_exp_f32 v21, v21\n"
      "v_exp_f32 v22, v22\n"
      "v_exp_f32 v23, v23\n"
      "v_exp_f32 v36, v36\n"
      "v_exp_f32 v37, v37\n"
      "v_exp_f32 v38, v38\n"
      "v_exp_f32 v39, v39\n"
      "v_cvt_pk_bf16_f32 v16, v16, v17\n"
      "v_cvt_pk_bf16_f32 v17, v18, v19\n"
      "v_cvt_pk_bf16_f32 v32, v20, v21\n"
      "v_cvt_pk_bf16_f32 v33, v22, v23\n"
      "v_cvt_pk_bf16_f32 v34, v36, v37\n"
      "v_cvt_pk_bf16_f32 v35, v38, v39\n"
      "v_cndmask_b32_e64 v28, 0, v16, s[62:63]\n"
      "v_cndmask_b32_e64 v29, 0, v17, s[62:63]\n"
      "v_cndmask_b32_e64 v30, v16, 0, s[62:63]\n"
      "v_cndmask_b32_e64 v31, v17, 0, s[62:63]\n"
      "s_waitcnt vmcnt(0)\n"
      "v_mfma_f32_16x16x32_bf16 v[20:23], v[44:47], v[32:35], 0\n"
      "v_mfma_f32_16x16x32_bf16 v[36:39], v[8:11], v[32:35], 0\n"
      "v_mfma_f32_16x16x32_bf16 v[40:43], v[12:15], v[32:35], 0\n"
      "v_and_b32 v27, 0x80, v0\n"
      "v_lshlrev_b32 v27, 3, v27\n"
      "v_and_or_b32 v27, v0, s61, v27\n"
      "s_lshl_b32 s44, s57, 10\n"
      "v_add_u32 v26, s44, v0\n"
      "s_add_u32 s39, s39, 786432\n"
      "v_mfma_f32_16x16x16_bf16 v[20:23], v[44:45], v[28:29], v[20:23]\n"
      "v_mfma_f32_16x16x16_bf16 v[36:39], v[24:25], v[28:29], v[36:39]\n"
      "v_mfma_f32_16x16x16_bf16 v[40:43], v[24:25], v[30:31], v[40:43]\n"
      "ds_read_b128 v[8:11], v7\n"
      "ds_read_b128 v[12:15], v7 offset:1024\n"
      "s_nop 3\n"
      "v_rcp_f32 v16, v20\n"
      "s_nop 0\n"
      "v_pk_mul_f32 v[36:37], v[36:37], v[16:17] op_sel_hi:[1,0]\n"
      "v_pk_mul_f32 v[38:39], v[38:39], v[16:17] op_sel_hi:[1,0]\n"
      "v_pk_mul_f32 v[40:41], v[40:41], v[16:17] op_sel_hi:[1,0]\n"
      "v_pk_mul_f32 v[42:43], v[42:43], v[16:17] op_sel_hi:[1,0]\n"
      "v_cvt_pk_bf16_f32 v16, v36, v37\n"
      "v_cvt_pk_bf16_f32 v17, v38, v39\n"
      "v_cvt_pk_bf16_f32 v18, v40, v41\n"
      "v_cvt_pk_bf16_f32 v19, v42, v43\n"
      "ds_write_b128 v26, v[16:19] offset:0\n"
      "s_waitcnt lgkmcnt(0)\n"
      "s_barrier\n"
      "ds_read_b128 v[44:47], v27 offset:0\n"
      "ds_read_b128 v[20:23], v27 offset:2048\n"
      "ds_read_b128 v[28:31], v0 offset:0\n"
      "ds_read_b128 v[32:35], v0 offset:1024\n"
      "ds_read_b128 v[36:39], v0 offset:2048\n"
      "ds_read_b128 v[40:43], v0 offset:3072\n"
      "s_add_u32 s44, s36, s50\n"
      "s_add_u32 s45, s44, s38\n"
      "s_add_u32 s44, s44, s37\n"
      "s_lshl_b32 s46, s57, 9\n"
      "s_sub_u32 s45, s45, s46\n"
      "s_waitcnt lgkmcnt(5)\n"
      "v_smfmac_f32_16x16x64_bf16 v[64:67], v[44:47], v[200:207], v1\n"
      "s_waitcnt lgkmcnt(4)\n"
      "v_smfmac_f32_16x16x64_bf16 v[64:67], v[20:23], v[208:215], v1\n"
      "ds_read_b128 v[16:19], v7 offset:8192\n"
      "ds_read_b128 v[20:23], v7 offset:9216\n"
      "s_waitcnt lgkmcnt(5)\n"
      "v_smfmac_f32_16x16x64_bf16 v[48:51], v[28:31], v[104:111], v1\n"
      "s_waitcnt lgkmcnt(4)\n"
      "v_smfmac_f32_16x16x64_bf16 v[48:51], v[32:35], v[112:119], v1\n"
      "s_waitcnt lgkmcnt(3)\n"
      "v_smfmac_f32_16x16x64_bf16 v[48:51], v[36:39], v[120:127], v1\n"
      "v_mov_b32 v68, v64\n"
      "v_mov_b32 v69, v65\n"
      "s_waitcnt lgkmcnt(2)\n"
      "v_smfmac_f32_16x16x64_bf16 v[48:51], v[40:43], v[128:135], v1\n"
      "v_mov_b32 v70, v66\n"
      "v_mov_b32 v71, v67\n"
      "v_smfmac_f32_16x16x64_bf16 v[60:63], v[28:31], v[8:15], v1\n"
      "ds_read_b128 v[8:11], v7 offset:16384\n"
      "ds_read_b128 v[12:15], v7 offset:17408\n"
      "v_smfmac_f32_16x16x64_bf16 v[52:55], v[28:31], v[136:143], v1\n"
      "v_permlane32_swap_b32 v64, v68\n"
      "v_permlane32_swap_b32 v65, v69\n"
      "v_smfmac_f32_16x16x64_bf16 v[52:55], v[32:35], v[144:151], v1\n"
      "v_permlane32_swap_b32 v66, v70\n"
      "v_permlane32_swap_b32 v67, v71\n"
      "v_smfmac_f32_16x16x64_bf16 v[52:55], v[36:39], v[152:159], v1\n"
      "v_pk_add_f32 v[64:65], v[64:65], v[68:69]\n"
      "v_pk_add_f32 v[66:67], v[66:67], v[70:71]\n"
      "v_smfmac_f32_16x16x64_bf16 v[52:55], v[40:43], v[160:167], v1\n"
      "v_and_b32 v68, 0xfffffeff, v3\n"
      "v_cvt_pk_bf16_f32 v64, v64, v65\n"
      "s_waitcnt lgkmcnt(2)\n"
      "v_smfmac_f32_16x16x64_bf16 v[60:63], v[32:35], v[16:23], v1\n"
      "ds_read_b128 v[16:19], v7 offset:24576\n"
      "ds_read_b128 v[20:23], v7 offset:25600\n"
      "v_smfmac_f32_16x16x64_bf16 v[56:59], v[28:31], v[168:175], v1\n"
      "v_cvt_pk_bf16_f32 v65, v66, v67\n"
      "buffer_store_dwordx2 v[64:65], v68, s[24:27], s45 offen sc1\n"
      "v_smfmac_f32_16x16x64_bf16 v[56:59], v[32:35], v[176:183], v1\n"
      "v_cvt_pk_bf16_f32 v48, v48, v49\n"
      "v_cvt_pk_bf16_f32 v49, v50, v51\n"
      "v_smfmac_f32_16x16x64_bf16 v[56:59], v[36:39], v[184:191], v1\n"
      "v_smfmac_f32_16x16x64_bf16 v[56:59], v[40:43], v[192:199], v1\n"
      "v_cvt_pk_bf16_f32 v50, v52, v53\n"
      "v_cvt_pk_bf16_f32 v51, v54, v55\n"
      "s_waitcnt lgkmcnt(2)\n"
      "v_smfmac_f32_16x16x64_bf16 v[60:63], v[36:39], v[8:15], v1\n"
      "v_permlane16_swap_b32 v48, v50\n"
      "v_permlane16_swap_b32 v49, v51\n"
      "s_waitcnt lgkmcnt(0)\n"
      "v_smfmac_f32_16x16x64_bf16 v[60:63], v[40:43], v[16:23], v1\n"
      "buffer_store_dwordx4 v[48:51], v0, s[24:27], s44 offen sc1\n"
      "v_cvt_pk_bf16_f32 v56, v56, v57\n"
      "v_cvt_pk_bf16_f32 v57, v58, v59\n"
      "s_nop 4\n"
      "v_cvt_pk_bf16_f32 v58, v60, v61\n"
      "v_cvt_pk_bf16_f32 v59, v62, v63\n"
      "s_nop 0\n"
      "v_permlane16_swap_b32 v56, v58\n"
      "v_permlane16_swap_b32 v57, v59\n"
      "buffer_store_dwordx4 v[56:59], v0, s[24:27], s44 offen offset:1024 sc1\n"
      "ds_read_b64 v[68:69], v3 offset:11520\n"
      "buffer_store_dwordx2 v[24:25], v3, s[24:27], s40 offen offset:2048 sc1\n"
      "v_mov_b32 v64, -1\n"
      "v_mov_b32 v65, -1\n"
      "v_mov_b32 v66, -1\n"
      "v_mov_b32 v67, -1\n"
      "s_waitcnt lgkmcnt(0)\n"
      "buffer_store_dwordx2 v[68:69], v3, s[24:27], s40 offen sc1\n"
      "s_add_u32 s40, s40, 131072\n"
      "s_cmp_ge_u32 s57, 2\n"
      "s_cbranch_scc1 L_nokt_%=\n"
      "s_add_u32 s44, s35, 1\n"
      "s_cmp_lt_u32 s44, s41\n"
      "s_cbranch_scc1 L_kvokl_%=\n"
      "s_lshl_b32 s47, s58, 5\n"
      "s_add_u32 s47, s47, s34\n"
      "s_lshl_b32 s47, s47, 8\n"
      "s_add_u32 s47, s47, 1024\n"
      "L_kvpolll_%=:\n"
      "buffer_load_dword v70, off, s[24:27], s47 sc1\n"
      "s_waitcnt vmcnt(0)\n"
      "v_readfirstlane_b32 s41, v70\n"
      "s_nop 1\n"
      "s_cmp_lt_u32 s44, s41\n"
      "s_cbranch_scc1 L_kvokl_%=\n"
      "s_sub_u32 s42, s42, 1\n"
      "s_cbranch_scc1 L_abort_%=\n"
      "s_sleep 4\n"
      "s_branch L_kvpolll_%=\n"
      "L_kvokl_%=:\n"
      "s_lshl_b32 s44, s57, 12\n"
      "s_add_u32 s45, s44, s39\n"
      "s_add_u32 m0, s44, 13568\n"
      "s_nop 0\n"
      "buffer_load_dwordx4 v0, s[24:27], s45 offen offset:0 sc1 lds\n"
      "buffer_load_dwordx4 v0, s[24:27], s45 offen offset:1024 sc1 lds\n"
      "buffer_load_dwordx4 v0, s[24:27], s45 offen offset:2048 sc1 lds\n"
      "buffer_load_dwordx4 v0, s[24:27], s45 offen offset:3072 sc1 lds\n"
      "L_nokt_%=:\n"
      "s_cmp_eq_u32 s43, -1\n"
      "s_cbranch_scc1 L_nored0_%=\n"
      "v_lshrrev_b32 v16, 8, v0\n"
      "v_mul_u32_u24 v16, s59, v16\n"
      "v_and_b32 v17, 0xff, v0\n"
      "v_add_u32 v16, v16, v17\n"
      "v_and_b32 v17, 0x70, v0\n"
      "ds_read_b128 v[20:23], v17 offset:21760\n"
      "v_lshlrev_b32 v17, 1, v17\n"
      "v_lshrrev_b32 v24, 8, v0\n"
      "v_lshl_add_u32 v24, v24, 3, v17\n"
      "s_cmp_eq_u32 s57, 2\n"
      "s_cbranch_scc1 L_d1o_%=\n"
      "s_sleep 7\n"
      "s_branch L_gpoll_%=\n"
      "L_d1o_%=:\n"
      "s_sleep 4\n"
      "L_gpoll_%=:\n"
      "s_add_u32 s44, s36, s50\n"
      "s_add_u32 s44, s44, s56\n"
      "buffer_load_dwordx4 v[28:31], v16, s[24:27], s44 offen sc1\n"
      "s_add_u32 s44, s44, s60\n"
      "buffer_load_dwordx4 v[32:35], v16, s[24:27], s44 offen sc1\n"
      "s_add_u32 s44, s44, s68\n"
      "buffer_load_dwordx4 v[36:39], v16, s[24:27], s44 offen sc1\n"
      "s_add_u32 s44, s44, s60\n"
      "buffer_load_dwordx4 v[40:43], v16, s[24:27], s44 offen sc1\n"
      "s_add_u32 s44, s44, s68\n"
      "buffer_load_dwordx4 v[44:47], v16, s[24:27], s44 offen sc1\n"
      "s_add_u32 s44, s44, s60\n"
      "buffer_load_dwordx4 v[48:51], v16, s[24:27], s44 offen sc1\n"
      "s_add_u32 s44, s44, s68\n"
      "buffer_load_dwordx4 v[52:55], v16, s[24:27], s44 offen sc1\n"
      "s_add_u32 s44, s44, s60\n"
      "buffer_load_dwordx4 v[56:59], v16, s[24:27], s44 offen sc1\n"
      "s_add_u32 s44, s36, s66\n"
      "s_add_u32 s45, s44, s53\n"
      "s_add_u32 s44, s44, s52\n"
      "s_waitcnt lgkmcnt(0)\n"
      "s_waitcnt vmcnt(7)\n"
      "v_mfma_f32_16x16x32_bf16 v[60:63], v[28:31], v[20:23], 0\n"
      "s_waitcnt vmcnt(6)\n"
      "v_mfma_f32_16x16x32_bf16 v[60:63], v[32:35], v[20:23], v[60:63]\n"
      "s_waitcnt vmcnt(5)\n"
      "v_mfma_f32_16x16x32_bf16 v[60:63], v[36:39], v[20:23], v[60:63]\n"
      "s_waitcnt vmcnt(4)\n"
      "v_mfma_f32_16x16x32_bf16 v[60:63], v[40:43], v[20:23], v[60:63]\n"
      "s_waitcnt vmcnt(3)\n"
      "v_mfma_f32_16x16x32_bf16 v[60:63], v[44:47], v[20:23], v[60:63]\n"
      "s_waitcnt vmcnt(2)\n"
      "v_mfma_f32_16x16x32_bf16 v[60:63], v[48:51], v[20:23], v[60:63]\n"
      "s_waitcnt vmcnt(1)\n"
      "v_mfma_f32_16x16x32_bf16 v[60:63], v[52:55], v[20:23], v[60:63]\n"
      "s_waitcnt vmcnt(0)\n"
      "v_mfma_f32_16x16x32_bf16 v[60:63], v[56:59], v[20:23], v[60:63]\n"
      "s_nop 7\n"
      "v_cmp_u_f32 vcc, v60, v61\n"
      "v_cmp_u_f32_e64 s[64:65], v62, v63\n"
      "v_cvt_pk_bf16_f32 v18, v60, v61\n"
      "v_cvt_pk_bf16_f32 v19, v62, v63\n"
      "s_or_b64 vcc, vcc, s[64:65]\n"
      "s_cbranch_vccnz L_gslow_%=\n"
      "L_gok_%=:\n"
      "buffer_store_dwordx2 v[18:19], v24, s[24:27], s44 offen sc1\n"
      "s_mov_b32 exec_lo, 0xffff\n"
      "s_mov_b32 exec_hi, 0\n"
      "buffer_store_dwordx4 v[64:67], v0, s[24:27], s45 offen sc1\n"
      "s_mov_b64 exec, -1\n"
      "L_nored_%=:\n"
      "s_barrier\n"
      "s_cmp_lg_u32 s57, 0\n"
      "s_cbranch_scc1 L_nokm_%=\n"
      "s_add_u32 s44, s35, 1\n"
      "v_mov_b32 v70, s44\n"
      "buffer_store_dword v70, off, s[24:27], s54 sc1\n"
      "L_nokm_%=:\n"
      "s_add_u32 s47, s36, s51\n"
      "s_add_u32 s48, s47, s38\n"
      "s_add_u32 s47, s47, s37\n"
      "buffer_store_dwordx4 v[64:67], v0, s[24:27], s47 offen sc1\n"
      "buffer_store_dwordx4 v[64:67], v0, s[24:27], s47 offen offset:1024 sc1\n"
      "s_mov_b32 exec_lo, 0xffff\n"
      "s_mov_b32 exec_hi, 0\n"
      "buffer_store_dwordx4 v[64:67], v0, s[24:27], s48 offen sc1\n"
      "s_mov_b64 exec, -1\n"
      "s_lshl_b32 s44, s57, 10\n"
      "s_add_u32 s44, s44, s36\n"
      "s_add_u32 s44, s44, s52\n"
      "s_add_u32 s44, s44, 1769472\n"
      "s_add_u32 s45, s44, 4096\n"
      "s_add_u32 s46, s44, 16384\n"
      "v_mov_b64 v[36:37], 0\n"
      "v_mov_b64 v[38:39], 0\n"
      "s_add_u32 s35, s35, 1\n"
      "s_mov_b32 s51, s50\n"
      "s_add_u32 s50, s50, 589824\n"
      "s_cmp_eq_u32 s50, 1769472\n"
      "s_cselect_b32 s50, 0, s50\n"
      "s_mov_b32 s53, s52\n"
      "s_add_u32 s52, s52, 32768\n"
      "s_cmp_eq_u32 s52, 98304\n"
      "s_cselect_b32 s52, 0, s52\n"
      "v_mov_b64 v[16:17], 0\n"
      "v_mov_b64 v[18:19], 0\n"
      "v_mov_b64 v[20:21], 0\n"
      "v_mov_b64 v[22:23], 0\n"
      "v_mov_b64 v[24:25], 0\n"
      "v_mov_b64 v[26:27], 0\n"
      "ds_read_b128 v[40:43], v5 offset:0\n"
      "ds_read_b128 v[44:47], v5 offset:1024\n"
      "ds_read_b128 v[48:51], v5 offset:2048\n"
      "ds_read_b128 v[52:55], v5 offset:3072\n"
      "ds_read_b128 v[56:59], v5 offset:16384\n"
      "ds_read_b128 v[60:63], v5 offset:17408\n"
      "ds_read_b128 v[8:11], v5 offset:18432\n"
      "ds_read_b128 v[12:15], v5 offset:19456\n"
      "s_sleep 6\n"
      "L_hpoll_%=:\n"
      "buffer_load_dwordx4 v[28:31], v0, s[24:27], s44 offen sc1\n"
      "buffer_load_dwordx4 v[32:35], v0, s[24:27], s45 offen sc1\n"
      "s_cmp_lg_u32 s57, 0\n"
      "s_cbranch_scc1 L_h2_%=\n"
      "buffer_load_dwordx4 v[36:39], v0, s[24:27], s46 offen sc1\n"
      "L_h2_%=:\n"
      "s_waitcnt vmcnt(0)\n"
      "ds_write_b128 v6, v[28:31] offset:0\n"
      "ds_write_b128 v6, v[32:35] offset:512\n"
      "s_cmp_lg_u32 s57, 0\n"
      "s_cbranch_scc1 L_h3_%=\n"
      "ds_write_b128 v6, v[36:39] offset:1024\n"
      "L_h3_%=:\n"
      "v_max3_u32 v64, v28, v29, v30\n"
      "v_max3_u32 v65, v31, v32, v33\n"
      "v_max3_u32 v66, v34, v35, v36\n"
      "v_max3_u32 v67, v37, v38, v39\n"
      "v_max3_u32 v64, v64, v65, v66\n"
      "v_max_u32 v64, v64, v67\n"
      "v_cmp_eq_u32 vcc, -1, v64\n"
      "s_cbranch_vccnz L_hretry_%=\n"
      "s_waitcnt lgkmcnt(0)\n"
      "s_barrier\n"
      "s_branch L_loop_%=\n"
      "L_nored0_%=:\n"
      "s_cmp_ge_u32 s57, 2\n"
      "s_cbranch_scc1 L_nored1_%=\n"
      "s_waitcnt vmcnt(4)\n"
      "s_branch L_nored_%=\n"
      "L_nored1_%=:\n"
      "s_waitcnt vmcnt(0)\n"
      "s_branch L_nored_%=\n"
      "L_hretry_%=:\n"
      "s_mov_b32 s95, 1\n"
      "s_sub_u32 s42, s42, 1\n"
      "s_cbranch_scc1 L_abort_%=\n"
      "s_branch L_hpoll_%=\n"
      "L_gslow_%=:\n"
      "v_max3_u32 v17, v28, v29, v30\n"
      "v_max3_u32 v17, v17, v31, v32\n"
      "v_max3_u32 v17, v17, v33, v34\n"
      "v_max3_u32 v17, v17, v35, v36\n"
      "v_max3_u32 v17, v17, v37, v38\n"
      "v_max3_u32 v17, v17, v39, v40\n"
      "v_max3_u32 v17, v17, v41, v42\n"
      "v_max3_u32 v17, v17, v43, v44\n"
      "v_max3_u32 v17, v17, v45, v46\n"
      "v_max3_u32 v17, v17, v47, v48\n"
      "v_max3_u32 v17, v17, v49, v50\n"
      "v_max3_u32 v17, v17, v51, v52\n"
      "v_max3_u32 v17, v17, v53, v54\n"
      "v_max3_u32 v17, v17, v55, v56\n"
      "v_max3_u32 v17, v17, v57, v58\n"
      "v_max_u32 v17, v17, v59\n"
      "v_min_u32 v18, -2, v18\n"
      "v_min_u32 v19, -2, v19\n"
      "v_cmp_eq_u32 vcc, -1, v17\n"
      "s_cbranch_vccz L_gok_%=\n"
      "s_mov_b32 s94, 1\n"
      "s_sub_u32 s42, s42, 1\n"
      "s_cbranch_scc1 L_abort_%=\n"
      "s_branch L_gpoll_%=\n"
      "L_final_%=:\n"
      "buffer_store_dwordx2 v[20:21], v3, s[24:27], s40 offen sc1\n"
      "buffer_store_dwordx2 v[24:25], v3, s[24:27], s40 offen offset:2048 sc1\n"
      "s_waitcnt vmcnt(0) lgkmcnt(0)\n"
      "s_barrier\n"
      "s_cmp_lg_u32 s57, 0\n"
      "s_cbranch_scc1 L_end_%=\n"
      "s_mov_b32 exec_lo, 1\n"
      "s_mov_b32 exec_hi, 0\n"
      "v_mov_b32 v20, 64\n"
      "buffer_store_dword v20, off, s[24:27], s54 sc1\n"
      "s_branch L_end_%=\n"
      "L_abort_%=:\n"
      "s_mov_b32 exec_lo, 1\n"
      "s_mov_b32 exec_hi, 0\n"
      "v_mov_b32 v20, 1\n"
      "s_mov_b32 s44, 61440\n"
      "buffer_store_dword v20, off, s[24:27], s44 sc1\n"
      "L_end_%=:\n"
      "s_waitcnt vmcnt(0)\n"
      "s_mov_b64 exec, -1\n"
      :: "s"(__builtin_amdgcn_kernarg_segment_ptr()), "s"(b), "s"(t), "s"(wave)
      : "memory", "vcc", "scc",
      "v6", "v7", "v8", "v9", "v10", "v11", "v12", "v13", "v14", "v15", "v16", "v17", "v18", "v19", "v20", "v21",
      "v22", "v23", "v24", "v25", "v26", "v27", "v28", "v29", "v30", "v31", "v32", "v33", "v34", "v35", "v36", "v37",
      "v38", "v39", "v40", "v41", "v42", "v43", "v44", "v45", "v46", "v47", "v48", "v49", "v50", "v51", "v52", "v53",
      "v54", "v55", "v56", "v57", "v58", "v59", "v60", "v61", "v62", "v63", "v64", "v65", "v66", "v67", "v68", "v69",
      "v70", "v71", "v72", "v73", "v74", "v75", "v76", "v77", "v78", "v79", "v80", "v81", "v82", "v83", "v84", "v85",
      "v86", "v87", "v88", "v89", "v90", "v91", "v92", "v93", "v94", "v95", "v96", "v97", "v98", "v99", "v100", "v101",
      "v102", "v103", "v104", "v105", "v106", "v107", "v108", "v109", "v110", "v111", "v112", "v113", "v114", "v115", "v116", "v117",
      "v118", "v119", "v120", "v121", "v122", "v123", "v124", "v125", "v126", "v127", "v128", "v129", "v130", "v131", "v132", "v133",
      "v134", "v135", "v136", "v137", "v138", "v139", "v140", "v141", "v142", "v143", "v144", "v145", "v146", "v147", "v148", "v149",
      "v150", "v151", "v152", "v153", "v154", "v155", "v156", "v157", "v158", "v159", "v160", "v161", "v162", "v163", "v164", "v165",
      "v166", "v167", "v168", "v169", "v170", "v171", "v172", "v173", "v174", "v175", "v176", "v177", "v178", "v179", "v180", "v181",
      "v182", "v183", "v184", "v185", "v186", "v187", "v188", "v189", "v190", "v191", "v192", "v193", "v194", "v195", "v196", "v197",
      "v198", "v199", "v200", "v201", "v202", "v203", "v204", "v205", "v206", "v207", "v208", "v209", "v210", "v211", "v212", "v213",
      "v214", "v215", "v216", "v217", "v218", "v219", "v220", "v221", "v222", "v223", "v224", "v225", "v226", "v227", "v228", "v229",
      "v230", "v231", "v232", "v233", "v234", "v235", "v236", "v237", "v238", "v239", "v240", "v241", "v242", "v243", "v244", "v245",
      "v246", "v247", "v248", "v249", "v250", "v251", "v252", "v253", "v254", "v255", "a0", "a1", "a2", "a3", "a4", "a5",
      "a6", "a7", "a8", "a9", "a10", "a11", "a12", "a13", "a14", "a15", "a16", "a17", "a18", "a19", "a20", "a21",
      "a22", "a23", "a24", "a25", "a26", "a27", "a28", "a29", "a30", "a31", "a32", "a33", "a34", "a35", "a36", "a37",
      "a38", "a39", "a40", "a41", "a42", "a43", "a44", "a45", "a46", "a47", "a48", "a49", "a50", "a51", "a52", "a53",
      "a54", "a55", "a56", "a57", "a58", "a59", "a60", "a61", "a62", "a63", "a64", "a65", "a66", "a67", "a68", "a69",
      "a70", "a71", "a72", "a73", "a74", "a75", "a76", "a77", "a78", "a79", "a80", "a81", "a82", "a83", "a84", "a85",
      "a86", "a87", "a88", "a89", "a90", "a91", "a92", "a93", "a94", "a95", "a96", "a97", "a98", "a99", "a100", "a101",
      "a102", "a103", "a104", "a105", "a106", "a107", "a108", "a109", "a110", "a111", "a112", "a113", "a114", "a115", "a116", "a117",
      "a118", "a119", "a120", "a121", "a122", "a123", "a124", "a125", "a126", "a127", "a128", "a129", "a130", "a131", "a132", "a133",
      "a134", "a135", "a136", "a137", "a138", "a139", "a140", "a141", "a142", "a143", "a144", "a145", "a146", "a147", "a148", "a149",
      "a150", "a151", "a152", "a153", "a154", "a155", "a156", "a157", "a158", "a159", "a160", "a161", "a162", "a163", "a164", "a165",
      "a166", "a167", "a168", "a169", "a170", "a171", "a172", "a173", "a174", "a175", "a176", "a177", "a178", "a179", "a180", "a181",
      "a182", "a183", "a184", "a185", "a186", "a187", "a188", "a189", "a190", "a191", "a192", "a193", "a194", "a195", "a196", "a197",
      "a198", "a199", "a200", "a201", "a202", "a203", "a204", "a205", "a206", "a207", "a208", "a209", "a210", "a211", "a212", "a213",
      "a214", "a215", "a216", "a217", "a218", "a219", "a220", "a221", "a222", "a223", "a224", "a225", "a226", "a227", "a228", "a229",
      "a230", "a231", "a232", "a233", "a234", "a235", "a236", "a237", "a238", "a239", "a240", "a241", "a242", "a243", "a244", "a245",
      "a246", "a247", "a248", "a249", "a250", "a251", "a252", "a253", "a254", "a255", "s24", "s25", "s26", "s27", "s28", "s29",
      "s30", "s31", "s34", "s35", "s36", "s37", "s38", "s39", "s40", "s41", "s42", "s43", "s44", "s45", "s46", "s47",
      "s48", "s49", "s50", "s51", "s52", "s53", "s54", "s55", "s56", "s57", "s58", "s59", "s60", "s61", "s62", "s63",
      "s64", "s65", "s66", "s67", "s68", "s69", "s70", "s71", "s72", "s73", "s74", "s75", "s76", "s77", "s78", "s79",
      "s80", "s81", "s82", "s83", "s84", "s85", "s86", "s87", "s88", "s89", "s90", "s91", "s92", "s93", "s94", "s95",
      "s96", "s97", "s98", "s99"
    );
    return;
  }
  const int u = t - 32, h = u & 31, grp = u >> 5;
  if (grp == 0) {
    if (wave == 0) probe_polarity(prm, b, h, lane);
    role_gate(prm, b * 32 + h, wave, lane);
    {
      const unsigned long long t0 = __builtin_amdgcn_s_memrealtime();
      while (__builtin_amdgcn_s_memrealtime() - t0 < RG_DELAY) __builtin_amdgcn_s_sleep(8);
    }
    role_regroup(prm, b, h, wave, lane);
    role_p2(prm, b, h, wave, lane, tl_id);
    return;
  }
  if (grp == 1) role_p1<true>(prm, b, h, wave, lane, tl_id);
  else {
    const unsigned long long t0 = __builtin_amdgcn_s_memrealtime();
    while (__builtin_amdgcn_s_memrealtime() - t0 < P1_DELAY) __builtin_amdgcn_s_sleep(8);
    role_p1<false>(prm, b, h, wave, lane, tl_id);
  }
  const int slice = grp == 2 ? h % 18 : (h < 12 ? 14 + (h & 3) : (h - 12) % 14);
  role_p3(prm, slice, b, wave, lane, tl_id);
}

extern "C" void kernel_launch(void* const* d_in, const int* in_sizes, int n_in, void* d_out, int out_size, void* d_ws, size_t ws_size, hipStream_t stream) {
  (void)in_sizes; (void)n_in; (void)out_size; (void)ws_size;
  Params prm;
  prm.p[0] = d_in[2]; prm.p[1] = d_in[3]; prm.p[2] = d_in[4]; prm.p[3] = d_in[5]; prm.p[4] = d_in[1];
  prm.p[5] = d_in[9]; prm.p[6] = d_in[10]; prm.p[7] = d_in[11]; prm.p[8] = d_in[12]; prm.p[9] = d_in[8];
  prm.p[10] = d_ws; prm.p[11] = d_in[0]; prm.p[12] = d_in[14]; prm.p[13] = d_out;
  (void)hipFuncSetAttribute((const void*)gated_mix_kernel, hipFuncAttributeMaxDynamicSharedMemorySize, LDS_BYTES);
  (void)hipMemsetAsync(d_ws, 0, CTL_BYTES, stream);
  hipLaunchKernelGGL(gated_mix_kernel, dim3(256), dim3(256), LDS_BYTES, stream, prm);
}
```
